```python
import torch

def _softmax(x, axis=-1):
    return torch.softmax(x, dim=axis)
def _gelu(x):
    return torch.nn.functional.gelu(x, approximate="tanh")
def _silu(x):
    return torch.nn.functional.silu(x)
def _sigmoid(x):
    return torch.sigmoid(x)
def _layernorm(x, axis=-1, eps=1e-5):
    m = x.mean(dim=axis, keepdim=True)
    v = x.var(dim=axis, keepdim=True, unbiased=False)
    return (x - m) / torch.sqrt(v + eps)
def _scatter_add(tgt, idx, src, axis=0):
    return tgt.clone().index_add_(axis, idx, src)
def _segment_sum(src, seg, n):
    out = torch.zeros((n,) + src.shape[1:], dtype=src.dtype)
    return out.index_add_(0, seg, src)
def _pad(x, pw):
    flat = tuple(v for pair in reversed(pw) for v in pair)
    return torch.nn.functional.pad(x, flat)

kernel_name = 'gated_mix__sliding_block__dense_activation_relu__prompt'

def setup_inputs():

    _w = (lambda a: (lambda t: t.to(torch.bfloat16) if t.is_floating_point() else t)(torch.as_tensor(a)))
    def _A_setup():
        B, S, D = 1, 2048, 512
        return {
            "x": _w((lambda *s: torch.randn(*s, dtype=torch.bfloat16) * 0.05)(B, S, D)),
            "wqkv": _w((lambda *s: torch.randn(*s, dtype=torch.bfloat16) * 0.8)(D, 3 * D)),
            "wo": _w((lambda *s: torch.randn(*s, dtype=torch.bfloat16) * 0.05)(D, D)),
        }
    _A_in = _A_setup()

    def _B_setup():
        B, S, D, Dff = 1, 2048, 512, 1376
        return {
            "x": _w((lambda *s: torch.randn(*s, dtype=torch.bfloat16) * 0.05)(B, S, D)),
            "w1": _w((lambda *s: torch.randn(*s, dtype=torch.bfloat16) * 0.05)(D, Dff)),
            "w2": _w((lambda *s: torch.randn(*s, dtype=torch.bfloat16) * 0.05)(Dff, D)),
        }
    _B_in = _B_setup()

    return {
        "A_x": _A_in["x"],
        "A_wqkv": _A_in["wqkv"],
        "A_wo": _A_in["wo"],
        "B_x": _B_in["x"],
        "B_w1": _B_in["w1"],
        "B_w2": _B_in["w2"],
        "gate_w": _w((lambda *s: torch.randn(*s, dtype=torch.bfloat16) * 0.05)(512) * 16.0),
    }

def reference(A_x, A_wqkv, A_wo, B_x, B_w1, B_w2, gate_w):
    def _A_ref(x, wqkv, wo):
        B, S, D = x.shape
        H = 32; Dh = D // H; W = 256
        qkv = (lambda x, *d: x.permute(*d))((x @ wqkv).reshape(B, S, 3, H, Dh), 2, 0, 3, 1, 4)
        q, k, v = qkv[0], qkv[1], qkv[2]
        scores = torch.einsum("bhqd,bhkd->bhqk", q, k) * (Dh ** -0.5)
        idx = torch.arange(S)
        mask = (idx[:, None] - idx[None, :] >= 0) & (idx[:, None] - idx[None, :] < W)
        scores = torch.where(mask[None, None], scores, -1e9)
        p = _softmax(scores, axis=-1)
        o = torch.einsum("bhqk,bhkd->bhqd", p, v)
        return (lambda x, *d: x.permute(*d))(o, 0, 2, 1, 3).reshape(B, S, D) @ wo

    def _B_ref(x, w1, w2):
        h = torch.einsum("bsd,df->bsf", x, w1)
        zero = torch.zeros_like(h)
        h = torch.maximum(h, zero)
        return torch.einsum("bsf,fd->bsd", h, w2)

    x = A_x
    g = _sigmoid((x * gate_w).sum(-1, keepdims=True))
    a = _A_ref(x, A_wqkv, A_wo)
    b = _B_ref(x, B_w1, B_w2)
    return g * a + (1 - g) * b
```

```cpp
#include <hip/hip_runtime.h>
#include <hip/hip_bf16.h>
#include <stdint.h>

typedef short bf16x8 __attribute__((ext_vector_type(8)));
typedef short s16x4 __attribute__((ext_vector_type(4)));
typedef short v4i16_t __attribute__((ext_vector_type(4)));
typedef float f32x4 __attribute__((ext_vector_type(4)));
typedef float f32x16 __attribute__((ext_vector_type(16)));
typedef unsigned u32x4 __attribute__((ext_vector_type(4)));
typedef unsigned u32x2 __attribute__((ext_vector_type(2)));
typedef __attribute__((address_space(3))) const char* lds_cptr;
typedef __attribute__((address_space(3))) char* lds_ptr;
#define DI __device__ __forceinline__
#define MFMA16(a, b, c) __builtin_amdgcn_mfma_f32_16x16x32_bf16((a), (b), (c), 0, 0, 0)
#define MFMA32(a, b, c) __builtin_amdgcn_mfma_f32_32x32x16_bf16((a), (b), (c), 0, 0, 0)

constexpr int S_ = 2048, D_ = 512, DFF = 1376, NQKV = 1536;
constexpr int KB_H = 86, KB_Z = 118;
constexpr size_t WS_QKV = 0;
constexpr size_t WS_Z = 6u << 20;
constexpr size_t WS_G = 16u << 20;
constexpr float QSCALE = 0.25f * 1.4426950408889634f;

DI unsigned cvtpk(float lo, float hi) { unsigned r; asm("v_cvt_pk_bf16_f32 %0, %1, %2" : "=v"(r) : "v"(lo), "v"(hi)); return r; }
DI void glds16(const void* sbase, unsigned voff, unsigned lds_base) {
    unsigned sv;
    asm volatile("s_mov_b32 %0, m0\n\ts_mov_b32 m0, %3\n\ts_nop 0\n\tglobal_load_lds_dwordx4 %1, %2\n\ts_mov_b32 m0, %0"
                 : "=&s"(sv) : "v"(voff), "s"(sbase), "s"(lds_base) : "memory");
}
DI s16x4 vtr(lds_cptr p) { return __builtin_bit_cast(s16x4, __builtin_amdgcn_ds_read_tr16_b64_v4i16((__attribute__((address_space(3))) v4i16_t*)p)); }
DI bf16x8 cat8(s16x4 lo, s16x4 hi) { return __builtin_shufflevector(lo, hi, 0, 1, 2, 3, 4, 5, 6, 7); }
#define WAIT_VM(n) asm volatile("s_waitcnt vmcnt(" #n ")" ::: "memory")
#define BAR() do { asm volatile("s_waitcnt lgkmcnt(0)" ::: "memory"); __builtin_amdgcn_s_barrier(); asm volatile("" ::: "memory"); } while (0)

DI void phase1(const __hip_bfloat16* Ax, const __hip_bfloat16* Bx, const __hip_bfloat16* wqkv, const __hip_bfloat16* w1,
               const __hip_bfloat16* gate_w, char* ws, char* lds, int bid) {
    const int tid = threadIdx.x, lane = tid & 63;
    const int wid = __builtin_amdgcn_readfirstlane(tid >> 6);
    const int x = bid & 7, j = bid >> 3;
    const bool isffn = x >= 4;
    const int rt = (x & 1) * 8 + (j & 7), ct = ((x >> 1) & 1) * 4 + (j >> 3);
    const char* X = (const char*)(isffn ? Bx : Ax) + (size_t)rt * 128 * 1024;
    const int ldw = isffn ? DFF * 2 : NQKV * 2;
    const int u0 = isffn ? (12 * ct < 74 ? 12 * ct : 74) : 12 * ct;
    const int uvalid = 12 * ct;
    const char* W = (const char*)(isffn ? w1 : wqkv) + u0 * 32;
    const unsigned lds0 = (unsigned)(uintptr_t)lds;
    const int r8 = lane >> 3, c8 = lane & 7;
    const unsigned xoff = (unsigned)((8 * wid + r8) * 1024 + ((c8 ^ (((wid & 1) << 2) | (r8 >> 1))) << 4));
    const int fw = ((r8 >> 1) & 1) | ((wid & 1) << 1);
    const unsigned woff = (unsigned)((8 * wid + r8) * ldw + ((((c8 >> 1) ^ fw) << 5) | ((c8 & 1) << 4)));
    auto stage = [&](int s, int slot) {
        const unsigned d = lds0 + slot * 40960 + wid * 1024;
        const char* xs = X + s * 128;
        const char* wsrc = W + (size_t)s * 64 * ldw;
        glds16(xs, xoff, d);
        glds16(xs + 64 * 1024, xoff, d + 8192);
        glds16(wsrc, woff, d + 16384);
        glds16(wsrc + 128, woff, d + 16384 + 8192);
        glds16(wsrc + 256, woff, d + 16384 + 16384);
    };
    stage(0, 0); stage(1, 1);
    const bool do_gate = !isffn && (wid == ((ct >> 2) << 2));
    if (wid == 0) { *(u32x4*)(lds + 122880 + lane * 16) = *(const u32x4*)((const char*)gate_w + lane * 16); }
    const int wr = wid >> 2, wc = wid & 3, fr = lane & 15, g = lane >> 4, q = fr >> 2, p = fr & 3;
    const unsigned xa0 = (unsigned)((wr * 64 + fr) * 128 + ((g ^ (fr >> 1)) << 4));
    unsigned wa[3];
#pragma unroll
    for (int i = 0; i < 3; ++i) { const int u = 3 * wc + i, cb = u >> 2, cp = u & 3; const int fx = (q >> 1) | ((g & 1) << 1);
        wa[i] = (unsigned)(16384 + (cb * 8 + g) * 1024 + q * 128 + ((cp ^ fx) << 5) + p * 8); }
    f32x4 acc[4][3];
#pragma unroll
    for (int m = 0; m < 4; ++m)
#pragma unroll
        for (int n = 0; n < 3; ++n) acc[m][n] = (f32x4){0.f, 0.f, 0.f, 0.f};
    f32x4 accg = {0.f, 0.f, 0.f, 0.f};
    int slot = 0;
    for (int s = 0; s < 8; ++s) {
        if (s < 7) WAIT_VM(5); else WAIT_VM(0);
        BAR();
        if (s + 2 < 8) { int ns = slot + 2; if (ns >= 3) ns -= 3; stage(s + 2, ns); }
        const lds_cptr L = (lds_cptr)lds + slot * 40960;
#pragma unroll
        for (int kk = 0; kk < 2; ++kk) {
            bf16x8 xf[4], wf[3];
#pragma unroll
            for (int m = 0; m < 4; ++m) xf[m] = *(const __attribute__((address_space(3))) bf16x8*)(L + ((xa0 ^ (kk * 64)) + m * 2048));
#pragma unroll
            for (int n = 0; n < 3; ++n) wf[n] = cat8(vtr(L + wa[n] + kk * 4096), vtr(L + wa[n] + kk * 4096 + 512));
#pragma unroll
            for (int m = 0; m < 4; ++m)
#pragma unroll
                for (int n = 0; n < 3; ++n) acc[m][n] = MFMA16(wf[n], xf[m], acc[m][n]);
            if (do_gate) {
                const bf16x8 gf = *(const __attribute__((address_space(3))) bf16x8*)((lds_cptr)lds + 122880 + (s * 64 + kk * 32 + g * 8) * 2);
                const int mt = ct & 3;
                const bf16x8 xg = mt == 0 ? xf[0] : mt == 1 ? xf[1] : mt == 2 ? xf[2] : xf[3];
                accg = MFMA16(gf, xg, accg);
            }
        }
        ++slot; if (slot == 3) slot = 0;
    }
    const int tok0 = rt * 128 + wr * 64;
    if (do_gate && g == 0) ((float*)(ws + WS_G))[tok0 + (ct & 3) * 16 + fr] = accg[0];
#pragma unroll
    for (int n = 0; n < 3; ++n) {
        const int u = u0 + 3 * wc + n;
        if (u < uvalid) continue;
#pragma unroll
        for (int m = 0; m < 4; ++m) {
            f32x4 v = acc[m][n];
            char* dst;
            if (isffn) {
                v[0] = fmaxf(v[0], 0.f); v[1] = fmaxf(v[1], 0.f); v[2] = fmaxf(v[2], 0.f); v[3] = fmaxf(v[3], 0.f);
                dst = ws + WS_Z + ((size_t)((tok0 >> 4) + m) * KB_Z + u) * 512;
            } else {
                if (u < 32) v *= QSCALE;
                dst = ws + WS_QKV + ((size_t)u * 2048 + tok0 + m * 16) * 32;
            }
            u32x2 o = {cvtpk(v[0], v[1]), cvtpk(v[2], v[3])};
            *(u32x2*)(dst + fr * 32 + g * 8) = o;
        }
    }
}

DI void phase2(char* ws, char* lds, int bid) {
    const int tid = threadIdx.x, lane = tid & 63;
    const int wid = __builtin_amdgcn_readfirstlane(tid >> 6);
    const int x = bid & 7, j = bid >> 3;
    const int head = 4 * x + (j & 3), rb = j >> 2;
    const unsigned lds0 = (unsigned)(uintptr_t)lds;
    const char* Qg = ws + WS_QKV + ((size_t)head * 2048) * 32;
    const char* Kg = Qg + (size_t)32 * 2048 * 32;
    const char* Vg = Kg + (size_t)32 * 2048 * 32;
    const int key0 = rb * 256 - 256;
    {
        const int lo = rb == 0 ? 0 : key0;
        const unsigned vo = (unsigned)(wid * 1024 + lane * 16);
        glds16(Kg + (size_t)lo * 32, vo, lds0 + wid * 1024);
        glds16(Kg + (size_t)(key0 + 256) * 32, vo, lds0 + 8192 + wid * 1024);
        glds16(Vg + (size_t)lo * 32, vo, lds0 + 16384 + wid * 1024);
        glds16(Vg + (size_t)(key0 + 256) * 32, vo, lds0 + 16384 + 8192 + wid * 1024);
    }
    if (tid < 16) *(unsigned*)(lds + 32768 + tid * 4) = 0x3f803f80u;
    const int r32 = lane & 31, h = lane >> 5;
    const int q0 = rb * 256 + wid * 32;
    const bf16x8 qf = *(const bf16x8*)(Qg + (size_t)(q0 + r32) * 32 + h * 16);
    const float glog = ((const float*)(ws + WS_G))[q0 + r32];
    (void)glog;
    WAIT_VM(0);
    BAR();
    const int t0 = rb == 0 ? 8 - wid : 0;
    const lds_cptr Kl = (lds_cptr)lds + (wid * 32 + r32) * 32 + h * 16;
    const int i16 = lane & 15, grp = (lane >> 4) & 1;
    const unsigned vbase = grp ? 32768u : (unsigned)(16384 + (wid * 32 + 4 * h + (i16 >> 2)) * 32 + (i16 & 3) * 8);
    const unsigned vstep = grp ? 0u : 1u;
    const float NEG = -__builtin_inff();
    const int aa = r32 - 4 * h;
    f32x16 zero;
#pragma unroll
    for (int r = 0; r < 16; ++r) zero[r] = 0.f;
    float mx = NEG;
    for (int t = t0; t < 9; ++t) {
        const bf16x8 kf = *(const __attribute__((address_space(3))) bf16x8*)(Kl + t * 1024);
        f32x16 s = MFMA32(kf, qf, zero);
        if (t == 0) {
#pragma unroll
            for (int r = 0; r < 16; ++r) { const int b = (r & 3) + 8 * (r >> 2); if (!(b > aa)) s[r] = NEG; }
        } else if (t == 8) {
#pragma unroll
            for (int r = 0; r < 16; ++r) { const int b = (r & 3) + 8 * (r >> 2); if (b > aa) s[r] = NEG; }
        }
#pragma unroll
        for (int r = 0; r < 16; r += 2) mx = fmaxf(fmaxf(mx, s[r]), s[r + 1]);
    }
    { auto rr = __builtin_amdgcn_permlane32_swap(__float_as_uint(mx), __float_as_uint(mx), false, false);
      mx = fmaxf(__uint_as_float(rr[0]), __uint_as_float(rr[1])); }
    f32x16 nm;
#pragma unroll
    for (int r = 0; r < 16; ++r) nm[r] = -mx;
    f32x16 o = zero;
    for (int t = t0; t < 9; ++t) {
        const bf16x8 kf = *(const __attribute__((address_space(3))) bf16x8*)(Kl + t * 1024);
        f32x16 s = MFMA32(kf, qf, nm);
        if (t == 0) {
#pragma unroll
            for (int r = 0; r < 16; ++r) { const int b = (r & 3) + 8 * (r >> 2); if (!(b > aa)) s[r] = NEG; }
        } else if (t == 8) {
#pragma unroll
            for (int r = 0; r < 16; ++r) { const int b = (r & 3) + 8 * (r >> 2); if (b > aa) s[r] = NEG; }
        }
#pragma unroll
        for (int r = 0; r < 16; ++r) s[r] = __builtin_amdgcn_exp2f(s[r]);
        const lds_cptr vp = (lds_cptr)lds + vbase + vstep * (unsigned)(t * 1024);
#pragma unroll
        for (int ss = 0; ss < 2; ++ss) {
            u32x4 pw = {cvtpk(s[8 * ss], s[8 * ss + 1]), cvtpk(s[8 * ss + 2], s[8 * ss + 3]), cvtpk(s[8 * ss + 4], s[8 * ss + 5]), cvtpk(s[8 * ss + 6], s[8 * ss + 7])};
            const lds_cptr v0 = vp + vstep * (unsigned)(ss * 512);
            const bf16x8 vf = cat8(vtr(v0), vtr(v0 + vstep * 256u));
            o = MFMA32(vf, __builtin_bit_cast(bf16x8, pw), o);
        }
    }
    const float inv = 1.0f / o[8];
    u32x4 ow = {cvtpk(o[0] * inv, o[1] * inv), cvtpk(o[2] * inv, o[3] * inv), cvtpk(o[4] * inv, o[5] * inv), cvtpk(o[6] * inv, o[7] * inv)};
    const int tok = q0 + r32;
    *(u32x4*)(ws + WS_Z + ((size_t)(tok >> 4) * KB_Z + KB_H + head) * 512 + (tok & 15) * 32 + h * 16) = ow;
}

constexpr int NS3 = 8;
DI void phase3(const __hip_bfloat16* w2, const __hip_bfloat16* wo, __hip_bfloat16* out, char* ws, char* lds, int bid) {
    const int tid = threadIdx.x, lane = tid & 63;
    const int wid = __builtin_amdgcn_readfirstlane(tid >> 6);
    const int x = bid & 7, j = bid >> 3;
    const int mt = x * 4 + (j & 3), nt = j >> 2;
    const unsigned lds0 = (unsigned)(uintptr_t)lds;
    const char* Zb = ws + WS_Z + (size_t)(mt * 4 + (wid >> 1)) * KB_Z * 512 + (wid & 1) * 1024;
    const int r8 = lane >> 3, c8 = lane & 7;
    const int fw = ((r8 >> 1) & 1) | ((wid & 1) << 1);
    const unsigned wcol = (unsigned)((((c8 >> 1) ^ fw) << 5) | ((c8 & 1) << 4));
    const int k = 8 * wid + r8;
    const int kperm = (k & ~12) | ((k & 4) << 1) | ((k & 8) >> 1);
    const unsigned woff2 = (unsigned)(k * 1024) + wcol, woffo = (unsigned)(kperm * 1024) + wcol;
    const unsigned woff2h = (unsigned)((k & 31) * 1024) + wcol;
    const char* W2 = (const char*)w2 + nt * 128;
    const char* Wo = (const char*)wo + nt * 128;
    auto stage = [&](int s, int slot) {
        const unsigned d = lds0 + slot * 16384 + wid * 1024;
        if (s < 22) {
            const int kb = (s == 21) ? 84 - 2 * (wid & 1) : 4 * s;
            glds16(Zb + (size_t)kb * 512, (unsigned)lane * 16, d);
            glds16(W2 + (size_t)s * 65536, s == 21 ? woff2h : woff2, d + 8192);
        } else {
            glds16(Zb + (size_t)(KB_H + 4 * (s - 22)) * 512, (unsigned)lane * 16, d);
            glds16(Wo + (size_t)(s - 22) * 65536, woffo, d + 8192);
        }
    };
#pragma unroll
    for (int s = 0; s < NS3 - 1; ++s) stage(s, s);
    const int kh = wid >> 2, wr = (wid >> 1) & 1, wc = wid & 1, fr = lane & 15, g = lane >> 4, q = fr >> 2, p = fr & 3;
    const unsigned xa = (unsigned)((2 * wr * 4 + 2 * kh + (g >> 1)) * 512 + fr * 32 + (g & 1) * 16);
    unsigned wa[2];
#pragma unroll
    for (int n = 0; n < 2; ++n) { const int cp = 2 * wc + n; const int fx = (q >> 1) | ((g & 1) << 1);
        wa[n] = (unsigned)(8192 + (4 * kh + g) * 1024 + q * 128 + ((cp ^ fx) << 5) + p * 8); }
    f32x4 acc[2][2], accb[2][2];
#pragma unroll
    for (int m = 0; m < 2; ++m)
#pragma unroll
        for (int n = 0; n < 2; ++n) { acc[m][n] = (f32x4){0.f, 0.f, 0.f, 0.f}; accb[m][n] = acc[m][n]; }
    const float gl0 = ((const float*)(ws + WS_G))[mt * 64 + wr * 32 + fr], gl1 = ((const float*)(ws + WS_G))[mt * 64 + wr * 32 + 16 + fr];
    int slot = 0;
    for (int s = 0; s < 30; ++s) {
        if (s + NS3 - 2 < 30) WAIT_VM(12); else WAIT_VM(0);
        BAR();
        if (s + NS3 - 1 < 30) { int ns = slot + NS3 - 1; if (ns >= NS3) ns -= NS3; stage(s + NS3 - 1, ns); }
        if (s == 22) {
#pragma unroll
            for (int m = 0; m < 2; ++m)
#pragma unroll
                for (int n = 0; n < 2; ++n) { accb[m][n] = acc[m][n]; acc[m][n] = (f32x4){0.f, 0.f, 0.f, 0.f}; }
        }
        if (!(s == 21 && kh == 1)) {
            const lds_cptr L = (lds_cptr)lds + slot * 16384;
            bf16x8 xf[2], wf[2];
#pragma unroll
            for (int m = 0; m < 2; ++m) xf[m] = *(const __attribute__((address_space(3))) bf16x8*)(L + xa + m * 2048);
#pragma unroll
            for (int n = 0; n < 2; ++n) wf[n] = cat8(vtr(L + wa[n]), vtr(L + wa[n] + 512));
#pragma unroll
            for (int m = 0; m < 2; ++m)
#pragma unroll
                for (int n = 0; n < 2; ++n) acc[m][n] = MFMA16(wf[n], xf[m], acc[m][n]);
        }
        ++slot; if (slot == NS3) slot = 0;
    }
    const float g0 = 1.0f / (1.0f + __expf(-gl0)), g1 = 1.0f / (1.0f + __expf(-gl1));
    f32x4 c[2][2];
#pragma unroll
    for (int n = 0; n < 2; ++n) { c[0][n] = acc[0][n] * g0 + accb[0][n] * (1.0f - g0); c[1][n] = acc[1][n] * g1 + accb[1][n] * (1.0f - g1); }
    BAR();
    if (kh == 1) {
#pragma unroll
        for (int m = 0; m < 2; ++m)
#pragma unroll
            for (int n = 0; n < 2; ++n) *(f32x4*)(lds + (((wid & 3) * 4 + m * 2 + n) * 64 + lane) * 16) = c[m][n];
    }
    BAR();
    if (kh == 0) {
#pragma unroll
        for (int m = 0; m < 2; ++m)
#pragma unroll
            for (int n = 0; n < 2; ++n) {
                const f32x4 v = c[m][n] + *(const f32x4*)(lds + (((wid & 3) * 4 + m * 2 + n) * 64 + lane) * 16);
                u32x2 o = {cvtpk(v[0], v[1]), cvtpk(v[2], v[3])};
                *(u32x2*)((char*)out + (size_t)(mt * 64 + wr * 32 + m * 16 + fr) * 1024 + (nt * 64 + wc * 32 + n * 16 + g * 4) * 2) = o;
            }
    }
}

__global__ __launch_bounds__(512, 2) void k_phase1(const __hip_bfloat16* Ax, const __hip_bfloat16* Bx, const __hip_bfloat16* wqkv, const __hip_bfloat16* w1,
                                                  const __hip_bfloat16* gate_w, char* ws) {
    extern __shared__ __attribute__((aligned(16))) char lds[];
    phase1(Ax, Bx, wqkv, w1, gate_w, ws, lds, blockIdx.x);
}
__global__ __launch_bounds__(512, 2) void k_phase2(char* ws) {
    extern __shared__ __attribute__((aligned(16))) char lds[];
    phase2(ws, lds, blockIdx.x);
}
__global__ __launch_bounds__(512, 2) void k_phase3(const __hip_bfloat16* w2, const __hip_bfloat16* wo, __hip_bfloat16* out, char* ws) {
    extern __shared__ __attribute__((aligned(16))) char lds[];
    phase3(w2, wo, out, ws, lds, blockIdx.x);
}

extern "C" void kernel_launch(void* const* d_in, const int* in_sizes, int n_in,
                              void* d_out, int out_size, void* d_ws, size_t ws_size,
                              hipStream_t stream) {
    const __hip_bfloat16* Ax = (const __hip_bfloat16*)d_in[0];
    const __hip_bfloat16* wqkv = (const __hip_bfloat16*)d_in[1];
    const __hip_bfloat16* wo = (const __hip_bfloat16*)d_in[2];
    const __hip_bfloat16* Bx = (const __hip_bfloat16*)d_in[0];
    const __hip_bfloat16* w1 = (const __hip_bfloat16*)d_in[4];
    const __hip_bfloat16* w2 = (const __hip_bfloat16*)d_in[5];
    const __hip_bfloat16* gw = (const __hip_bfloat16*)d_in[6];
    char* ws = (char*)d_ws;
    hipFuncSetAttribute((const void*)k_phase1, hipFuncAttributeMaxDynamicSharedMemorySize, 123904);
    hipFuncSetAttribute((const void*)k_phase2, hipFuncAttributeMaxDynamicSharedMemorySize, 32832);
    hipFuncSetAttribute((const void*)k_phase3, hipFuncAttributeMaxDynamicSharedMemorySize, NS3 * 16384);
    k_phase1<<<256, 512, 123904, stream>>>(Ax, Bx, wqkv, w1, gw, ws);
    k_phase2<<<256, 512, 32832, stream>>>(ws);
    k_phase3<<<256, 512, NS3 * 16384, stream>>>(w2, wo, (__hip_bfloat16*)d_out, ws);
}
```

```cpp
#include <hip/hip_runtime.h>
#include <hip/hip_bf16.h>
#include <stdint.h>

typedef short bf16x8 __attribute__((ext_vector_type(8)));
typedef short s16x4 __attribute__((ext_vector_type(4)));
typedef short s16x2 __attribute__((ext_vector_type(2)));
typedef short v4i16_t __attribute__((ext_vector_type(4)));
typedef float f32x2 __attribute__((ext_vector_type(2)));
typedef float f32x4 __attribute__((ext_vector_type(4)));
typedef float f32x16 __attribute__((ext_vector_type(16)));
typedef __bf16 bf16x2_t __attribute__((ext_vector_type(2)));
typedef unsigned u32x4 __attribute__((ext_vector_type(4)));
typedef unsigned u32x2 __attribute__((ext_vector_type(2)));
typedef unsigned long long u64;
typedef __attribute__((address_space(3))) const char* lds_cptr;
typedef __attribute__((address_space(3))) char* lds_ptr;
#define DI __device__ __forceinline__
#define MFMA16(a, b, c) __builtin_amdgcn_mfma_f32_16x16x32_bf16((a), (b), (c), 0, 0, 0)
#define MFMA32(a, b, c) __builtin_amdgcn_mfma_f32_32x32x16_bf16((a), (b), (c), 0, 0, 0)
#define SF() __builtin_amdgcn_sched_barrier(0)

constexpr int DFF = 1376, NQKV = 1536;
constexpr int KB_H = 86, KB_Z = 118;
constexpr unsigned WS_KV = 0;
constexpr unsigned WS_Z = 6u << 20;
constexpr unsigned WS_G = 16u << 20;
constexpr unsigned WS_E = 17u << 20;
constexpr unsigned WS_F0 = WS_E + 4096, WS_F1 = WS_F0 + 32768, WS_F3 = WS_F1 + 32768;
constexpr unsigned WS_BYTES = 18u << 20;
constexpr unsigned DROP = 0xFFFFFF00u;
constexpr float QSCALE = 0.25f * 1.4426950408889634f;
constexpr int HS = 16384;
constexpr int WE = 24576;
constexpr int XR = 98304;

DI unsigned cvtpk(float lo, float hi) { f32x2 v = {lo, hi}; return __builtin_bit_cast(unsigned, __builtin_convertvector(v, bf16x2_t)); }
template <int IMM> DI void glds16_(const void* sbase, unsigned voff, unsigned lds_base) {
    asm volatile("s_add_u32 m0, %2, %3\n\ts_nop 0\n\tglobal_load_lds_dwordx4 %0, %1" ::"v"(voff), "s"(sbase), "s"(lds_base), "i"(IMM) : "memory", "scc");
}
template <int IMM> DI void glds16c_(const void* sbase, unsigned voff, unsigned lds_base) {
    asm volatile("s_add_u32 m0, %2, %3\n\ts_nop 0\n\tglobal_load_lds_dwordx4 %0, %1 sc1" ::"v"(voff), "s"(sbase), "s"(lds_base), "i"(IMM) : "memory", "scc");
}
#define glds16(sbase, voff, ldsb, imm) glds16_<(imm)>((sbase), (voff), (ldsb))
#define glds16c(sbase, voff, ldsb, imm) glds16c_<(imm)>((sbase), (voff), (ldsb))
DI u64 ld8c(const void* sbase, unsigned voff) { u64 r; asm volatile("global_load_dwordx2 %0, %1, %2 sc1 ; ASYNC" : "=v"(r) : "v"(voff), "s"(sbase) : "memory"); return r; }
DI u32x4 ld16c(const void* sbase, unsigned voff) { u32x4 r; asm volatile("global_load_dwordx4 %0, %1, %2 sc1 ; ASYNC" : "=v"(r) : "v"(voff), "s"(sbase) : "memory"); return r; }
DI u32x4 ld16p(const void* sbase, unsigned voff) { u32x4 r; asm volatile("global_load_dwordx4 %0, %1, %2 ; ASYNC" : "=v"(r) : "v"(voff), "s"(sbase) : "memory"); return r; }
DI unsigned touch8(const void* sbase, unsigned voff) { unsigned r; asm volatile("s_mov_b64 exec, 0xff\n\tglobal_load_dword %0, %1, %2 ; ASYNC\n\ts_mov_b64 exec, -1" : "=v"(r) : "v"(voff), "s"(sbase) : "memory"); return r; }
template <unsigned long long MASK> DI unsigned touchn(const void* sbase, unsigned voff) { unsigned r; asm volatile("s_mov_b64 exec, %3\n\tglobal_load_dword %0, %1, %2 ; ASYNC\n\ts_mov_b64 exec, -1" : "=v"(r) : "v"(voff), "s"(sbase), "s"(MASK) : "memory"); return r; }
DI s16x4 vtr(lds_cptr p) { return __builtin_bit_cast(s16x4, __builtin_amdgcn_ds_read_tr16_b64_v4i16((__attribute__((address_space(3))) v4i16_t*)p)); }
DI bf16x8 cat8(s16x4 lo, s16x4 hi) { return __builtin_shufflevector(lo, hi, 0, 1, 2, 3, 4, 5, 6, 7); }
DI bf16x8 ldsr16(lds_cptr p) { return *(const __attribute__((address_space(3))) bf16x8*)p; }
DI bf16x8 wfrag(lds_cptr p) { return cat8(vtr(p), vtr(p + 512)); }
DI bf16x8 wfrag64(lds_cptr p) { return cat8(vtr(p), vtr(p + 256)); }
DI bf16x8 asbf(u32x4 v) { return __builtin_bit_cast(bf16x8, v); }
#define STAMP(i) do {} while (0)
#define WAIT_VM(n) asm volatile("s_waitcnt vmcnt(" #n ")" ::: "memory")
#define BAR() do { asm volatile("s_waitcnt lgkmcnt(0)" ::: "memory"); __builtin_amdgcn_s_barrier(); asm volatile("" ::: "memory"); } while (0)
#define BARN(n) do { asm volatile("s_waitcnt lgkmcnt(" #n ")" ::: "memory"); __builtin_amdgcn_s_barrier(); asm volatile("" ::: "memory"); } while (0)
#define BAR0() do { asm volatile("" ::: "memory"); __builtin_amdgcn_s_barrier(); asm volatile("" ::: "memory"); } while (0)
#define PIN(x) asm volatile("; PIN %0" : "+v"(x))

DI void spin_flags(const char* ws, unsigned off, u64 v, u64 tag) {
#pragma clang loop unroll(disable)
    for (int it = 0; it < (1 << 16); ++it) {
        if (__all(v == tag)) break;
        __builtin_amdgcn_s_sleep(2);
        asm volatile("global_load_dwordx2 %0, %1, %2 sc1\n\ts_waitcnt vmcnt(0)" : "=v"(v) : "v"(off), "s"(ws) : "memory");
    }
}
DI void spin_flags3(const char* ws, unsigned off, u64 v, u64 tag, u64& a, u64& b, u64& c) {
    unsigned cnt;
    asm volatile("v_cmp_ne_u64 vcc, %4, %6\n\ts_nop 4\n\ts_cbranch_vccz .Lsd%=\n\t"
                 "global_load_dwordx2 %0, %5, %7 sc1\n\ts_sleep 3\n\tglobal_load_dwordx2 %1, %5, %7 sc1\n\ts_sleep 3\n\ts_movk_i32 %3, 0x4000\n"
                 ".Lsl%=:\n\t"
                 "global_load_dwordx2 %2, %5, %7 sc1\n\ts_waitcnt vmcnt(2)\n\tv_cmp_ne_u64 vcc, %0, %6\n\ts_nop 4\n\ts_cbranch_vccz .Lsd%=\n\ts_sleep 3\n\t"
                 "global_load_dwordx2 %0, %5, %7 sc1\n\ts_waitcnt vmcnt(2)\n\tv_cmp_ne_u64 vcc, %1, %6\n\ts_nop 4\n\ts_cbranch_vccz .Lsd%=\n\ts_sleep 3\n\t"
                 "global_load_dwordx2 %1, %5, %7 sc1\n\ts_waitcnt vmcnt(2)\n\tv_cmp_ne_u64 vcc, %2, %6\n\ts_nop 4\n\ts_cbranch_vccz .Lsd%=\n\ts_sleep 3\n\t"
                 "s_sub_u32 %3, %3, 1\n\ts_cbranch_scc0 .Lsl%=\n"
                 ".Lsd%=:\n\t; ASYNCQ %0 %1 %2"
                 : "=&v"(a), "=&v"(b), "=&v"(c), "=&s"(cnt) : "v"(v), "v"(off), "s"(tag), "s"(ws) : "vcc", "scc", "memory");
}
#define PINQ(a, b, c) asm volatile("; PINQ %0 %1 %2" : "+v"(a), "+v"(b), "+v"(c))
DI void splat_if(f32x16& v, int a, int b, float val) {
    float e0 = v[0], e1 = v[1], e2 = v[2], e3 = v[3], e4 = v[4], e5 = v[5], e6 = v[6], e7 = v[7];
    float e8 = v[8], e9 = v[9], e10 = v[10], e11 = v[11], e12 = v[12], e13 = v[13], e14 = v[14], e15 = v[15];
    asm volatile("s_cmp_lg_u32 %16, %17\n\ts_cbranch_scc1 .Lsk%=\n\ts_nop 15\n\ts_nop 15\n\t"
                 "v_mov_b32 %0, %18\n\tv_mov_b32 %1, %18\n\tv_mov_b32 %2, %18\n\tv_mov_b32 %3, %18\n\tv_mov_b32 %4, %18\n\tv_mov_b32 %5, %18\n\tv_mov_b32 %6, %18\n\tv_mov_b32 %7, %18\n\t"
                 "v_mov_b32 %8, %18\n\tv_mov_b32 %9, %18\n\tv_mov_b32 %10, %18\n\tv_mov_b32 %11, %18\n\tv_mov_b32 %12, %18\n\tv_mov_b32 %13, %18\n\tv_mov_b32 %14, %18\n\tv_mov_b32 %15, %18\n\t"
                 "s_nop 7\n.Lsk%=:"
                 : "+v"(e0), "+v"(e1), "+v"(e2), "+v"(e3), "+v"(e4), "+v"(e5), "+v"(e6), "+v"(e7), "+v"(e8), "+v"(e9), "+v"(e10), "+v"(e11), "+v"(e12), "+v"(e13), "+v"(e14), "+v"(e15)
                 : "s"(__builtin_amdgcn_readfirstlane(a)), "s"(b), "v"(val)
                 : "scc");
    v = (f32x16){e0, e1, e2, e3, e4, e5, e6, e7, e8, e9, e10, e11, e12, e13, e14, e15};
}
DI f32x16 qk(bf16x8 k, bf16x8 q, const f32x16& c) {
    f32x16 d;
    asm volatile("s_nop 1\n\tv_mfma_f32_32x32x16_bf16 %0, %1, %2, %3" : "=&v"(d) : "v"(k), "v"(q), "v"(c));
    return d;
}
DI u32x4 pair16(u32x2 a, u32x2 b) {
    auto r0 = __builtin_amdgcn_permlane16_swap(a[0], b[0], false, false);
    auto r1 = __builtin_amdgcn_permlane16_swap(a[1], b[1], false, false);
    return (u32x4){r0[0], r1[0], r0[1], r1[1]};
}
DI unsigned relu2(unsigned p) { const s16x2 z = {0, 0}; return __builtin_bit_cast(unsigned, __builtin_elementwise_max(__builtin_bit_cast(s16x2, p), z)); }
DI float sum_rows(float v) {
    auto a = __builtin_amdgcn_permlane16_swap(__float_as_uint(v), __float_as_uint(v), false, false);
    v = __uint_as_float(a[0]) + __uint_as_float(a[1]);
    auto b = __builtin_amdgcn_permlane32_swap(__float_as_uint(v), __float_as_uint(v), false, false);
    return __uint_as_float(b[0]) + __uint_as_float(b[1]);
}
#define ROT1 (((bj & 7) + 2 * (bj >> 3)) & 7)
#define ROT3 ((nt + 3 * (mt & 3) + 2 * (bx & 3)) % 10)
#define ROT4 ((nt + (mt & 3) + bx) & 3)
#define XLD ld16c
#define PRIOX(par) asm volatile("s_setprio 0\n\ts_cmp_lg_u32 %0, " #par "\n\ts_cbranch_scc1 .Lpr%=\n\ts_setprio 1\n.Lpr%=:" :: "s"(wid >> 2) : "scc")

__global__ __launch_bounds__(512, 2) void k_fused(const char* __restrict__ X, const char* __restrict__ wqkv, const char* __restrict__ wo, const char* __restrict__ w1,
                                                 const char* __restrict__ w2, const char* __restrict__ gate_w, char* __restrict__ out, char* __restrict__ ws) {
    extern __shared__ __attribute__((aligned(16))) char lds[];
    const int tid = threadIdx.x, lane = tid & 63;
    const int wid = __builtin_amdgcn_readfirstlane(tid >> 6);
    const int bx = blockIdx.x & 7, bj = blockIdx.x >> 3;
    const unsigned lds0 = (unsigned)(uintptr_t)lds;
    const lds_cptr L = (lds_cptr)lds;
    const __amdgpu_buffer_rsrc_t wsr = __builtin_amdgcn_make_buffer_rsrc(ws, 0, WS_BYTES, 0x00020000);
    STAMP(0);
    unsigned cpv;
    {
        u64 pc;
        asm volatile("s_getpc_b64 %0" : "=s"(pc));
        const unsigned ln = (unsigned)(lane * 8 + wid);
        asm volatile("global_load_dword %0, %1, %2 ; ASYNC" : "=v"(cpv) : "v"((ln < 164u ? ln : 164u - 1u) * 128u), "s"(pc) : "memory");
    }
    const int r8 = lane >> 3, c8 = lane & 7, r16 = lane >> 2, c4 = lane & 3;
    const int fr = lane & 15, g = lane >> 4, q4 = fr >> 2, p4 = fr & 3;
    const int wm = wid >> 1, wc = wid & 1;
    const unsigned ldsw = lds0 + wid * 1024;
    const int hi = wid >> 2;

    const int rt = (bx & 1) * 8 + (bj & 7), ct = (bx >> 1) * 4 + (bj >> 3);
    const int p0 = ct <= 11 ? 3 * ct : 2 * ct + 11;
    const int pcnt = ct < 11 ? 3 : 2;
    const int no0 = (int)((unsigned)(rt - 2) >> 31), no1 = (int)((unsigned)(rt - 1) >> 31);
    const int b0 = (rt - 2) * (1 - no0), b1 = (rt - 1) * (1 - no1), b2 = rt, t0 = no0 * (8 - 4 * rt - wm);
    const char* Xs = X + (size_t)rt * 128 * 1024;
    const char* As[3] = {wqkv + ct * 64, wqkv + 1024 + (ct & 1) * 1024 + (ct >> 1) * 128, wqkv + 1024 + (ct & 1) * 1024 + (ct >> 1) * 128 + 64};
    const char* Gs[3] = {w1 + p0 * 64, w1 + (p0 + 1) * 64, w1 + (p0 + 2 > 42 ? 42 : p0 + 2) * 64};
    const unsigned xoff = (unsigned)((8 * wid + r8) * 1024 + ((c8 ^ (((wid & 1) << 2) | (r8 >> 1))) << 4));
    const unsigned wsw = (unsigned)((c4 ^ ((r16 >> 3) << 1)) << 4);
    const int r1 = ROT1;
    const unsigned aoff = (unsigned)(r16 * (NQKV * 2)) + wsw;
    const unsigned goff = (unsigned)(r16 * (DFF * 2)) + wsw;
    const int wr0 = (wid & 3) * 16, wh = (wid >> 2) + r1;
#define dS(s, h) glds16(Xs + ((((s) + r1) & 7) << 7) + (h) * 65536, xoff, ldsw, XR + ((s) & 3) * HS + (h) * 8192)
#define dA(e, j) glds16(As[j] + (size_t)((((2 * (e) + wh) & 7) << 6) + wr0) * (NQKV * 2), aoff, ldsw, (e) * WE + (j) * 8192)
#define dG(e, j) glds16(Gs[j] + (size_t)((((2 * (e) + wh) & 7) << 6) + wr0) * (DFF * 2), goff, ldsw, (e) * WE + (j) * 8192)

    asm volatile("" :: "s"(Xs), "s"(As[0]), "v"(xoff), "v"(aoff));
    STAMP(28);
    dS(0, 0);
    dS(0, 1);
    STAMP(29);
    dA(0, 0);
    dA(0, 1);
    dA(0, 2);
    dS(1, 0);
    dS(1, 1);
    STAMP(26);
    u64 epoch; u32x4 gx, gw;
    dS(2, 0);
    dS(2, 1);
    STAMP(27);

    const int mt = bx * 4 + (bj & 3), nt = bj >> 2;
    const int krow = 8 * wid + r8;
    const unsigned swz = (unsigned)((((c8 >> 1) ^ (((r8 >> 1) & 1) | ((wid & 1) << 1))) << 5) | ((c8 & 1) << 4));
    const unsigned w2off = (unsigned)(krow * 1024) + swz;
    const unsigned wooff = (unsigned)(((krow & ~12) | ((krow & 4) << 1) | ((krow & 8) >> 1)) * 1024) + swz;
    const char* W2s = w2 + nt * 128;
    const char* Wos = wo + nt * 128;
    const unsigned t10 = wid >= 4 ? 0u : 1u;
    const int r3 = ROT3, r4 = ROT4;
#define PH(s) ((s) < 10 ? ((s) + r3 >= 10 ? (s) + r3 - 10 : (s) + r3) : ((s) == 10 ? 10 : (((s) - 11 + r4) & 3)))
#define dW(s, hs, h) do { \
        if ((s) == 10 && (h) == 1) glds16(W2s + (size_t)10 * 131072 + t10 * 65536, w2off, ldsw + t10 * 8192, (hs) * HS); \
        else if ((s) < 11) glds16(W2s + (size_t)PH(s) * 131072 + (h) * 65536, w2off, ldsw, (hs) * HS + (h) * 8192); \
        else glds16(Wos + (size_t)PH(s) * 131072 + (h) * 65536, wooff, ldsw, (hs) * HS + (h) * 8192); } while (0)

#define RWL(s, h) ld16p(((s) < 11 ? W2s : Wos) + (size_t)PH(s) * 131072 + (h) * 65536, (s) < 11 ? w2off : wooff)
    const unsigned wrb = (unsigned)(tid * 16);
    const unsigned xa = (unsigned)((wm * 32 + fr) * 128 + ((g ^ (fr >> 1)) << 4));
    const unsigned wa = (unsigned)((g * 8 + q4) * 64 + ((wc ^ (g & 1)) << 5) + p4 * 8);
    bf16x8 xf[16][2];
    bf16x8 wf[3][3];
    f32x4 acc[2][3], ac2[2][3];
#pragma unroll
    for (int m = 0; m < 2; ++m)
#pragma unroll
        for (int n = 0; n < 3; ++n) { acc[m][n] = (f32x4){0.f, 0.f, 0.f, 0.f}; ac2[m][n] = acc[m][n]; }

    WAIT_VM(4);
    BAR();
    PIN(cpv);
    STAMP(1);
    xf[0][0] = ldsr16(L + 98304 + ((xa ^ 0) + 0));
    xf[0][1] = ldsr16(L + 98304 + ((xa ^ 0) + 2048));
    wf[0][0] = wfrag64(L + 0 + wa);
    wf[0][1] = wfrag64(L + 8192 + wa);
    wf[0][2] = wfrag64(L + 16384 + wa);
    SF();
    xf[1][0] = ldsr16(L + 98304 + ((xa ^ 64) + 0));
    xf[1][1] = ldsr16(L + 98304 + ((xa ^ 64) + 2048));
    SF();
    wf[1][0] = wfrag64(L + 2048 + wa);
    wf[1][1] = wfrag64(L + 10240 + wa);
    wf[1][2] = wfrag64(L + 18432 + wa);
    SF();
    acc[0][0] = MFMA16(wf[0][0], xf[0][0], acc[0][0]);
    SF();
    wf[2][0] = wfrag64(L + 4096 + wa);
    SF();
    acc[1][0] = MFMA16(wf[0][0], xf[0][1], acc[1][0]);
    SF();
    wf[2][1] = wfrag64(L + 12288 + wa);
    dA(1, 0);
    SF();
    acc[0][1] = MFMA16(wf[0][1], xf[0][0], acc[0][1]);
    SF();
    wf[2][2] = wfrag64(L + 20480 + wa);
    dA(1, 1);
    SF();
    acc[1][1] = MFMA16(wf[0][1], xf[0][1], acc[1][1]);
    SF();
    dA(1, 2);
    SF();
    acc[0][2] = MFMA16(wf[0][2], xf[0][0], acc[0][2]);
    SF();
    dS(3, 0);
    SF();
    acc[1][2] = MFMA16(wf[0][2], xf[0][1], acc[1][2]);
    SF();
    dS(3, 1);
    SF();
    asm volatile("" : "+v"(acc[0][0]), "+v"(acc[0][1]), "+v"(acc[0][2]), "+v"(acc[1][0]), "+v"(acc[1][1]), "+v"(acc[1][2]));
    WAIT_VM(7);
    BARN(12);
    STAMP(2);
    acc[0][0] = MFMA16(wf[1][0], xf[1][0], acc[0][0]);
    SF();
    xf[2][0] = ldsr16(L + 114688 + ((xa ^ 0) + 0));
    xf[2][1] = ldsr16(L + 114688 + ((xa ^ 0) + 2048));
    xf[3][0] = ldsr16(L + 114688 + ((xa ^ 64) + 0));
    xf[3][1] = ldsr16(L + 114688 + ((xa ^ 64) + 2048));
    SF();
    acc[1][0] = MFMA16(wf[1][0], xf[1][1], acc[1][0]);
    SF();
    wf[0][0] = wfrag64(L + 6144 + wa);
    SF();
    acc[0][1] = MFMA16(wf[1][1], xf[1][0], acc[0][1]);
    SF();
    wf[0][1] = wfrag64(L + 14336 + wa);
    SF();
    acc[1][1] = MFMA16(wf[1][1], xf[1][1], acc[1][1]);
    SF();
    wf[0][2] = wfrag64(L + 22528 + wa);
    SF();
    acc[0][2] = MFMA16(wf[1][2], xf[1][0], acc[0][2]);
    SF();
    dS(4, 0);
    SF();
    acc[1][2] = MFMA16(wf[1][2], xf[1][1], acc[1][2]);
    SF();
    dS(4, 1);
    SF();
    asm volatile("" : "+v"(acc[0][0]), "+v"(acc[0][1]), "+v"(acc[0][2]), "+v"(acc[1][0]), "+v"(acc[1][1]), "+v"(acc[1][2]));
    WAIT_VM(4);
    BARN(6);
    STAMP(3);
    acc[0][0] = MFMA16(wf[2][0], xf[2][0], acc[0][0]);
    SF();
    xf[4][0] = ldsr16(L + 131072 + ((xa ^ 0) + 0));
    xf[4][1] = ldsr16(L + 131072 + ((xa ^ 0) + 2048));
    xf[5][0] = ldsr16(L + 131072 + ((xa ^ 64) + 0));
    xf[5][1] = ldsr16(L + 131072 + ((xa ^ 64) + 2048));
    SF();
    acc[1][0] = MFMA16(wf[2][0], xf[2][1], acc[1][0]);
    SF();
    wf[1][0] = wfrag64(L + 24576 + wa);
    SF();
    acc[0][1] = MFMA16(wf[2][1], xf[2][0], acc[0][1]);
    SF();
    wf[1][1] = wfrag64(L + 32768 + wa);
    SF();
    acc[1][1] = MFMA16(wf[2][1], xf[2][1], acc[1][1]);
    SF();
    wf[1][2] = wfrag64(L + 40960 + wa);
    SF();
    acc[0][2] = MFMA16(wf[2][2], xf[2][0], acc[0][2]);
    SF();
    dS(5, 0);
    SF();
    acc[1][2] = MFMA16(wf[2][2], xf[2][1], acc[1][2]);
    SF();
    dS(5, 1);
    SF();
    asm volatile("" : "+v"(acc[0][0]), "+v"(acc[0][1]), "+v"(acc[0][2]), "+v"(acc[1][0]), "+v"(acc[1][1]), "+v"(acc[1][2]));
    acc[0][0] = MFMA16(wf[0][0], xf[3][0], acc[0][0]);
    SF();
    wf[2][0] = wfrag64(L + 26624 + wa);
    SF();
    acc[1][0] = MFMA16(wf[0][0], xf[3][1], acc[1][0]);
    SF();
    wf[2][1] = wfrag64(L + 34816 + wa);
    SF();
    acc[0][1] = MFMA16(wf[0][1], xf[3][0], acc[0][1]);
    SF();
    wf[2][2] = wfrag64(L + 43008 + wa);
    SF();
    acc[1][1] = MFMA16(wf[0][1], xf[3][1], acc[1][1]);
    SF();
    dA(2, 0);
    SF();
    acc[0][2] = MFMA16(wf[0][2], xf[3][0], acc[0][2]);
    SF();
    dA(2, 1);
    SF();
    acc[1][2] = MFMA16(wf[0][2], xf[3][1], acc[1][2]);
    SF();
    dA(2, 2);
    SF();
    asm volatile("" : "+v"(acc[0][0]), "+v"(acc[0][1]), "+v"(acc[0][2]), "+v"(acc[1][0]), "+v"(acc[1][1]), "+v"(acc[1][2]));
    WAIT_VM(7);
    BARN(12);
    STAMP(4);
    acc[0][0] = MFMA16(wf[1][0], xf[4][0], acc[0][0]);
    SF();
    xf[6][0] = ldsr16(L + 147456 + ((xa ^ 0) + 0));
    xf[6][1] = ldsr16(L + 147456 + ((xa ^ 0) + 2048));
    xf[7][0] = ldsr16(L + 147456 + ((xa ^ 64) + 0));
    xf[7][1] = ldsr16(L + 147456 + ((xa ^ 64) + 2048));
    SF();
    acc[1][0] = MFMA16(wf[1][0], xf[4][1], acc[1][0]);
    SF();
    wf[0][0] = wfrag64(L + 28672 + wa);
    SF();
    acc[0][1] = MFMA16(wf[1][1], xf[4][0], acc[0][1]);
    SF();
    wf[0][1] = wfrag64(L + 36864 + wa);
    SF();
    acc[1][1] = MFMA16(wf[1][1], xf[4][1], acc[1][1]);
    SF();
    wf[0][2] = wfrag64(L + 45056 + wa);
    SF();
    acc[0][2] = MFMA16(wf[1][2], xf[4][0], acc[0][2]);
    SF();
    dS(6, 0);
    SF();
    acc[1][2] = MFMA16(wf[1][2], xf[4][1], acc[1][2]);
    SF();
    dS(6, 1);
    SF();
    asm volatile("" : "+v"(acc[0][0]), "+v"(acc[0][1]), "+v"(acc[0][2]), "+v"(acc[1][0]), "+v"(acc[1][1]), "+v"(acc[1][2]));
    acc[0][0] = MFMA16(wf[2][0], xf[5][0], acc[0][0]);
    SF();
    wf[1][0] = wfrag64(L + 30720 + wa);
    SF();
    acc[1][0] = MFMA16(wf[2][0], xf[5][1], acc[1][0]);
    SF();
    wf[1][1] = wfrag64(L + 38912 + wa);
    SF();
    acc[0][1] = MFMA16(wf[2][1], xf[5][0], acc[0][1]);
    SF();
    wf[1][2] = wfrag64(L + 47104 + wa);
    SF();
    acc[1][1] = MFMA16(wf[2][1], xf[5][1], acc[1][1]);
    SF();
    dA(3, 0);
    SF();
    acc[0][2] = MFMA16(wf[2][2], xf[5][0], acc[0][2]);
    SF();
    dA(3, 1);
    SF();
    acc[1][2] = MFMA16(wf[2][2], xf[5][1], acc[1][2]);
    SF();
    dA(3, 2);
    SF();
    asm volatile("" : "+v"(acc[0][0]), "+v"(acc[0][1]), "+v"(acc[0][2]), "+v"(acc[1][0]), "+v"(acc[1][1]), "+v"(acc[1][2]));
    WAIT_VM(5);
    BARN(12);
    STAMP(5);
    acc[0][0] = MFMA16(wf[0][0], xf[6][0], acc[0][0]);
    SF();
    xf[8][0] = ldsr16(L + 98304 + ((xa ^ 0) + 0));
    xf[8][1] = ldsr16(L + 98304 + ((xa ^ 0) + 2048));
    xf[9][0] = ldsr16(L + 98304 + ((xa ^ 64) + 0));
    xf[9][1] = ldsr16(L + 98304 + ((xa ^ 64) + 2048));
    SF();
    acc[1][0] = MFMA16(wf[0][0], xf[6][1], acc[1][0]);
    SF();
    wf[2][0] = wfrag64(L + 49152 + wa);
    dS(7, 0);
    SF();
    acc[0][1] = MFMA16(wf[0][1], xf[6][0], acc[0][1]);
    SF();
    wf[2][1] = wfrag64(L + 57344 + wa);
    dS(7, 1);
    SF();
    acc[1][1] = MFMA16(wf[0][1], xf[6][1], acc[1][1]);
    SF();
    wf[2][2] = wfrag64(L + 65536 + wa);
    epoch = ld8c(ws + WS_E, 0);
    SF();
    acc[0][2] = MFMA16(wf[0][2], xf[6][0], acc[0][2]);
    SF();
    gx = ld16p(Xs + ct * 64, (unsigned)((wid * 16 + fr) * 1024 + g * 16));
    SF();
    acc[1][2] = MFMA16(wf[0][2], xf[6][1], acc[1][2]);
    SF();
    gw = ld16p(gate_w + ct * 64, (unsigned)(g * 16));
    SF();
    asm volatile("" : "+v"(acc[0][0]), "+v"(acc[0][1]), "+v"(acc[0][2]), "+v"(acc[1][0]), "+v"(acc[1][1]), "+v"(acc[1][2]));
    acc[0][0] = MFMA16(wf[1][0], xf[7][0], acc[0][0]);
    SF();
    wf[0][0] = wfrag64(L + 51200 + wa);
    SF();
    acc[1][0] = MFMA16(wf[1][0], xf[7][1], acc[1][0]);
    SF();
    wf[0][1] = wfrag64(L + 59392 + wa);
    SF();
    acc[0][1] = MFMA16(wf[1][1], xf[7][0], acc[0][1]);
    SF();
    wf[0][2] = wfrag64(L + 67584 + wa);
    SF();
    acc[1][1] = MFMA16(wf[1][1], xf[7][1], acc[1][1]);
    SF();
    acc[0][2] = MFMA16(wf[1][2], xf[7][0], acc[0][2]);
    SF();
    acc[1][2] = MFMA16(wf[1][2], xf[7][1], acc[1][2]);
    SF();
    asm volatile("" : "+v"(acc[0][0]), "+v"(acc[0][1]), "+v"(acc[0][2]), "+v"(acc[1][0]), "+v"(acc[1][1]), "+v"(acc[1][2]));
    BAR0();
    STAMP(6);
    acc[0][0] = MFMA16(wf[2][0], xf[8][0], acc[0][0]);
    SF();
    xf[10][0] = ldsr16(L + 114688 + ((xa ^ 0) + 0));
    xf[10][1] = ldsr16(L + 114688 + ((xa ^ 0) + 2048));
    xf[11][0] = ldsr16(L + 114688 + ((xa ^ 64) + 0));
    xf[11][1] = ldsr16(L + 114688 + ((xa ^ 64) + 2048));
    SF();
    acc[1][0] = MFMA16(wf[2][0], xf[8][1], acc[1][0]);
    SF();
    wf[1][0] = wfrag64(L + 53248 + wa);
    SF();
    acc[0][1] = MFMA16(wf[2][1], xf[8][0], acc[0][1]);
    SF();
    wf[1][1] = wfrag64(L + 61440 + wa);
    SF();
    acc[1][1] = MFMA16(wf[2][1], xf[8][1], acc[1][1]);
    SF();
    wf[1][2] = wfrag64(L + 69632 + wa);
    SF();
    acc[0][2] = MFMA16(wf[2][2], xf[8][0], acc[0][2]);
    SF();
    dG(0, 0);
    SF();
    acc[1][2] = MFMA16(wf[2][2], xf[8][1], acc[1][2]);
    SF();
    dG(0, 1);
    SF();
    asm volatile("" : "+v"(acc[0][0]), "+v"(acc[0][1]), "+v"(acc[0][2]), "+v"(acc[1][0]), "+v"(acc[1][1]), "+v"(acc[1][2]));
    acc[0][0] = MFMA16(wf[0][0], xf[9][0], acc[0][0]);
    SF();
    wf[2][0] = wfrag64(L + 55296 + wa);
    SF();
    acc[1][0] = MFMA16(wf[0][0], xf[9][1], acc[1][0]);
    SF();
    wf[2][1] = wfrag64(L + 63488 + wa);
    SF();
    acc[0][1] = MFMA16(wf[0][1], xf[9][0], acc[0][1]);
    SF();
    wf[2][2] = wfrag64(L + 71680 + wa);
    SF();
    acc[1][1] = MFMA16(wf[0][1], xf[9][1], acc[1][1]);
    SF();
    acc[0][2] = MFMA16(wf[0][2], xf[9][0], acc[0][2]);
    SF();
    acc[1][2] = MFMA16(wf[0][2], xf[9][1], acc[1][2]);
    SF();
    dG(0, 2);
    SF();
    asm volatile("" : "+v"(acc[0][0]), "+v"(acc[0][1]), "+v"(acc[0][2]), "+v"(acc[1][0]), "+v"(acc[1][1]), "+v"(acc[1][2]));
    WAIT_VM(8);
    BAR0();
    STAMP(7);
    acc[0][0] = MFMA16(wf[1][0], xf[10][0], acc[0][0]);
    SF();
    xf[12][0] = ldsr16(L + 131072 + ((xa ^ 0) + 0));
    xf[12][1] = ldsr16(L + 131072 + ((xa ^ 0) + 2048));
    xf[13][0] = ldsr16(L + 131072 + ((xa ^ 64) + 0));
    xf[13][1] = ldsr16(L + 131072 + ((xa ^ 64) + 2048));
    SF();
    acc[1][0] = MFMA16(wf[1][0], xf[10][1], acc[1][0]);
    SF();
    wf[0][0] = wfrag64(L + 73728 + wa);
    SF();
    acc[0][1] = MFMA16(wf[1][1], xf[10][0], acc[0][1]);
    SF();
    wf[0][1] = wfrag64(L + 81920 + wa);
    SF();
    acc[1][1] = MFMA16(wf[1][1], xf[10][1], acc[1][1]);
    SF();
    wf[0][2] = wfrag64(L + 90112 + wa);
    SF();
    acc[0][2] = MFMA16(wf[1][2], xf[10][0], acc[0][2]);
    SF();
    dG(1, 0);
    SF();
    acc[1][2] = MFMA16(wf[1][2], xf[10][1], acc[1][2]);
    SF();
    dG(1, 1);
    SF();
    asm volatile("" : "+v"(acc[0][0]), "+v"(acc[0][1]), "+v"(acc[0][2]), "+v"(acc[1][0]), "+v"(acc[1][1]), "+v"(acc[1][2]));
    acc[0][0] = MFMA16(wf[2][0], xf[11][0], acc[0][0]);
    SF();
    wf[1][0] = wfrag64(L + 75776 + wa);
    SF();
    acc[1][0] = MFMA16(wf[2][0], xf[11][1], acc[1][0]);
    SF();
    wf[1][1] = wfrag64(L + 83968 + wa);
    SF();
    acc[0][1] = MFMA16(wf[2][1], xf[11][0], acc[0][1]);
    SF();
    wf[1][2] = wfrag64(L + 92160 + wa);
    SF();
    acc[1][1] = MFMA16(wf[2][1], xf[11][1], acc[1][1]);
    SF();
    acc[0][2] = MFMA16(wf[2][2], xf[11][0], acc[0][2]);
    SF();
    acc[1][2] = MFMA16(wf[2][2], xf[11][1], acc[1][2]);
    SF();
    dG(1, 2);
    SF();
    asm volatile("" : "+v"(acc[0][0]), "+v"(acc[0][1]), "+v"(acc[0][2]), "+v"(acc[1][0]), "+v"(acc[1][1]), "+v"(acc[1][2]));
    WAIT_VM(9);
    BARN(15);
    STAMP(8);
    acc[0][0] = MFMA16(wf[0][0], xf[12][0], acc[0][0]);
    SF();
    xf[14][0] = ldsr16(L + 147456 + ((xa ^ 0) + 0));
    xf[14][1] = ldsr16(L + 147456 + ((xa ^ 0) + 2048));
    xf[15][0] = ldsr16(L + 147456 + ((xa ^ 64) + 0));
    xf[15][1] = ldsr16(L + 147456 + ((xa ^ 64) + 2048));
    SF();
    acc[1][0] = MFMA16(wf[0][0], xf[12][1], acc[1][0]);
    SF();
    wf[2][0] = wfrag64(L + 77824 + wa);
    SF();
    acc[0][1] = MFMA16(wf[0][1], xf[12][0], acc[0][1]);
    SF();
    wf[2][1] = wfrag64(L + 86016 + wa);
    SF();
    acc[1][1] = MFMA16(wf[0][1], xf[12][1], acc[1][1]);
    SF();
    wf[2][2] = wfrag64(L + 94208 + wa);
    SF();
    acc[0][2] = MFMA16(wf[0][2], xf[12][0], acc[0][2]);
    SF();
    acc[1][2] = MFMA16(wf[0][2], xf[12][1], acc[1][2]);
    SF();
    dG(2, 0);
    SF();
    asm volatile("" : "+v"(acc[0][0]), "+v"(acc[0][1]), "+v"(acc[0][2]), "+v"(acc[1][0]), "+v"(acc[1][1]), "+v"(acc[1][2]));
    acc[0][0] = MFMA16(wf[1][0], xf[13][0], acc[0][0]);
    SF();
    wf[0][0] = wfrag64(L + 79872 + wa);
    SF();
    acc[1][0] = MFMA16(wf[1][0], xf[13][1], acc[1][0]);
    SF();
    wf[0][1] = wfrag64(L + 88064 + wa);
    SF();
    acc[0][1] = MFMA16(wf[1][1], xf[13][0], acc[0][1]);
    SF();
    wf[0][2] = wfrag64(L + 96256 + wa);
    SF();
    acc[1][1] = MFMA16(wf[1][1], xf[13][1], acc[1][1]);
    SF();
    acc[0][2] = MFMA16(wf[1][2], xf[13][0], acc[0][2]);
    SF();
    dG(2, 1);
    SF();
    acc[1][2] = MFMA16(wf[1][2], xf[13][1], acc[1][2]);
    SF();
    dG(2, 2);
    SF();
    asm volatile("" : "+v"(acc[0][0]), "+v"(acc[0][1]), "+v"(acc[0][2]), "+v"(acc[1][0]), "+v"(acc[1][1]), "+v"(acc[1][2]));
    WAIT_VM(6);
    BARN(12);
    STAMP(9);
    PIN(epoch); PIN(gx); PIN(gw);
    acc[0][0] = MFMA16(wf[2][0], xf[14][0], acc[0][0]);
    SF();
    wf[1][0] = wfrag64(L + 0 + wa);
    SF();
    acc[1][0] = MFMA16(wf[2][0], xf[14][1], acc[1][0]);
    SF();
    wf[1][1] = wfrag64(L + 8192 + wa);
    SF();
    acc[0][1] = MFMA16(wf[2][1], xf[14][0], acc[0][1]);
    SF();
    wf[1][2] = wfrag64(L + 16384 + wa);
    SF();
    acc[1][1] = MFMA16(wf[2][1], xf[14][1], acc[1][1]);
    SF();
    acc[0][2] = MFMA16(wf[2][2], xf[14][0], acc[0][2]);
    SF();
    dW(10, 9, 0);
    SF();
    acc[1][2] = MFMA16(wf[2][2], xf[14][1], acc[1][2]);
    SF();
    dW(10, 9, 1);
    SF();
    asm volatile("" : "+v"(acc[0][0]), "+v"(acc[0][1]), "+v"(acc[0][2]), "+v"(acc[1][0]), "+v"(acc[1][1]), "+v"(acc[1][2]));
    acc[0][0] = MFMA16(wf[0][0], xf[15][0], acc[0][0]);
    SF();
    wf[2][0] = wfrag64(L + 2048 + wa);
    SF();
    acc[1][0] = MFMA16(wf[0][0], xf[15][1], acc[1][0]);
    SF();
    wf[2][1] = wfrag64(L + 10240 + wa);
    SF();
    acc[0][1] = MFMA16(wf[0][1], xf[15][0], acc[0][1]);
    SF();
    wf[2][2] = wfrag64(L + 18432 + wa);
    SF();
    acc[1][1] = MFMA16(wf[0][1], xf[15][1], acc[1][1]);
    SF();
    acc[0][2] = MFMA16(wf[0][2], xf[15][0], acc[0][2]);
    SF();
    acc[1][2] = MFMA16(wf[0][2], xf[15][1], acc[1][2]);
    SF();
    const f32x4 ag = MFMA16(asbf(gw), asbf(gx), ((f32x4){0.f, 0.f, 0.f, 0.f}));
    SF();
    asm volatile("" : "+v"(acc[0][0]), "+v"(acc[0][1]), "+v"(acc[0][2]), "+v"(acc[1][0]), "+v"(acc[1][1]), "+v"(acc[1][2]));

    const u64 ep = ((u64)(unsigned)__builtin_amdgcn_readfirstlane((unsigned)(epoch >> 32)) << 32) | (unsigned)__builtin_amdgcn_readfirstlane((unsigned)epoch);
    const u64 tag = ep * 0x9E3779B97F4A7C15ull + 0x632BE59BD9B4E019ull;
    const u32x2 tag2 = {(unsigned)tag, (unsigned)(tag >> 32)};
    const int r32 = lane & 31, h = lane >> 5, aa = r32 - 4 * h;
    const unsigned zdst = WS_Z + (unsigned)((rt * 8 + wm * 2 + (g & 1)) * KB_Z * 512 + fr * 32 + (g >> 1) * 16 + (2 * p0 + wc) * 512);
    const unsigned kvdst = WS_KV + (unsigned)((rt * 16 + ct) * 16384 + (wc * 4 + wm) * 1024 + h * 512 + ((r32 ^ ((ct & 1) * 4 * h)) << 4));
    const int f0l = lane < 6 ? lane : 5;
    const int f0rt = rt - (f0l >> 1) < 0 ? 0 : rt - (f0l >> 1);
    const unsigned f0off = WS_F0 + (unsigned)((f0rt * 16 + (ct & 14) + (f0l & 1)) * 128);
    const unsigned f1off = WS_F1 + (unsigned)(((mt >> 1) * 16 + (lane & 15)) * 128);
    const unsigned f3off = WS_F3 + (unsigned)(((mt >> 1) * 16 + (lane & 15)) * 128);
    u64 fl;
    u32x4 qraw;
#define PK1(n, sc) pair16((u32x2){cvtpk(acc[0][n][0] * (sc), acc[0][n][1] * (sc)), cvtpk(acc[0][n][2] * (sc), acc[0][n][3] * (sc))}, \
                          (u32x2){cvtpk(acc[1][n][0] * (sc), acc[1][n][1] * (sc)), cvtpk(acc[1][n][2] * (sc), acc[1][n][3] * (sc))})
#define ST2(n) do { \
        const f32x4 v0 = ac2[0][n], v1 = ac2[1][n]; \
        __builtin_amdgcn_raw_buffer_store_b128(pair16((u32x2){relu2(cvtpk(v0[0], v0[1])), relu2(cvtpk(v0[2], v0[3]))}, (u32x2){relu2(cvtpk(v1[0], v1[1])), relu2(cvtpk(v1[2], v1[3]))}), wsr, \
                                               (n) < pcnt ? zdst + (n) * 1024u : DROP, 0, 16); } while (0)
#define FLAG(off) __builtin_amdgcn_raw_buffer_store_b64(tag2, wsr, tid == 0 ? (unsigned)(off) : DROP, 0, 16)

    BARN(12);
    ac2[0][0] = MFMA16(wf[1][0], xf[0][0], ac2[0][0]);
    SF();
    wf[0][0] = wfrag64(L + 4096 + wa);
    SF();
    ac2[1][0] = MFMA16(wf[1][0], xf[0][1], ac2[1][0]);
    SF();
    wf[0][1] = wfrag64(L + 12288 + wa);
    SF();
    ac2[0][1] = MFMA16(wf[1][1], xf[0][0], ac2[0][1]);
    SF();
    wf[0][2] = wfrag64(L + 20480 + wa);
    __builtin_amdgcn_raw_buffer_store_b128(PK1(1, 1.0f), wsr, kvdst, 0, 16);
    SF();
    ac2[1][1] = MFMA16(wf[1][1], xf[0][1], ac2[1][1]);
    SF();
    __builtin_amdgcn_raw_buffer_store_b128(PK1(2, 1.0f), wsr, kvdst + 8192u, 0, 16);
    SF();
    ac2[0][2] = MFMA16(wf[1][2], xf[0][0], ac2[0][2]);
    SF();
    qraw = PK1(0, QSCALE);
    SF();
    ac2[1][2] = MFMA16(wf[1][2], xf[0][1], ac2[1][2]);
    SF();
    asm volatile("" : "+v"(ac2[0][0]), "+v"(ac2[0][1]), "+v"(ac2[0][2]), "+v"(ac2[1][0]), "+v"(ac2[1][1]), "+v"(ac2[1][2]));
    ac2[0][0] = MFMA16(wf[2][0], xf[1][0], ac2[0][0]);
    SF();
    wf[1][0] = wfrag64(L + 6144 + wa);
    SF();
    ac2[1][0] = MFMA16(wf[2][0], xf[1][1], ac2[1][0]);
    SF();
    wf[1][1] = wfrag64(L + 14336 + wa);
    SF();
    ac2[0][1] = MFMA16(wf[2][1], xf[1][0], ac2[0][1]);
    SF();
    wf[1][2] = wfrag64(L + 22528 + wa);
    SF();
    ac2[1][1] = MFMA16(wf[2][1], xf[1][1], ac2[1][1]);
    SF();
    dG(3, 0);
    __builtin_amdgcn_raw_buffer_store_b32(__float_as_uint(ag[0]), wsr, g == 0 ? WS_G + (unsigned)((ct >> 2) * 32768 + (rt * 128 + wid * 16 + fr) * 16 + (ct & 3) * 4) : DROP, 0, 16);
    SF();
    ac2[0][2] = MFMA16(wf[2][2], xf[1][0], ac2[0][2]);
    SF();
    dG(3, 1);
    SF();
    ac2[1][2] = MFMA16(wf[2][2], xf[1][1], ac2[1][2]);
    SF();
    dG(3, 2);
    SF();
    asm volatile("" : "+v"(ac2[0][0]), "+v"(ac2[0][1]), "+v"(ac2[0][2]), "+v"(ac2[1][0]), "+v"(ac2[1][1]), "+v"(ac2[1][2]));
    WAIT_VM(11);
    BARN(2);
    STAMP(10);
    ac2[0][0] = MFMA16(wf[0][0], xf[2][0], ac2[0][0]);
    SF();
    wf[2][0] = wfrag64(L + 24576 + wa);
    SF();
    ac2[1][0] = MFMA16(wf[0][0], xf[2][1], ac2[1][0]);
    SF();
    wf[2][1] = wfrag64(L + 32768 + wa);
    SF();
    ac2[0][1] = MFMA16(wf[0][1], xf[2][0], ac2[0][1]);
    SF();
    wf[2][2] = wfrag64(L + 40960 + wa);
    SF();
    ac2[1][1] = MFMA16(wf[0][1], xf[2][1], ac2[1][1]);
    SF();
    ac2[0][2] = MFMA16(wf[0][2], xf[2][0], ac2[0][2]);
    SF();
    dW(0, 0, 0);
    SF();
    ac2[1][2] = MFMA16(wf[0][2], xf[2][1], ac2[1][2]);
    SF();
    dW(0, 0, 1);
    SF();
    asm volatile("" : "+v"(ac2[0][0]), "+v"(ac2[0][1]), "+v"(ac2[0][2]), "+v"(ac2[1][0]), "+v"(ac2[1][1]), "+v"(ac2[1][2]));
    ac2[0][0] = MFMA16(wf[1][0], xf[3][0], ac2[0][0]);
    SF();
    wf[0][0] = wfrag64(L + 26624 + wa);
    SF();
    ac2[1][0] = MFMA16(wf[1][0], xf[3][1], ac2[1][0]);
    SF();
    wf[0][1] = wfrag64(L + 34816 + wa);
    SF();
    ac2[0][1] = MFMA16(wf[1][1], xf[3][0], ac2[0][1]);
    SF();
    wf[0][2] = wfrag64(L + 43008 + wa);
    SF();
    ac2[1][1] = MFMA16(wf[1][1], xf[3][1], ac2[1][1]);
    SF();
    ac2[0][2] = MFMA16(wf[1][2], xf[3][0], ac2[0][2]);
    SF();
    ac2[1][2] = MFMA16(wf[1][2], xf[3][1], ac2[1][2]);
    SF();
    if (tid < 64) *(u32x4*)(lds + 159744 + tid * 16) = (u32x4){0x3f803f80u, 0x3f803f80u, 0x3f803f80u, 0x3f803f80u};
    SF();
    asm volatile("" : "+v"(ac2[0][0]), "+v"(ac2[0][1]), "+v"(ac2[0][2]), "+v"(ac2[1][0]), "+v"(ac2[1][1]), "+v"(ac2[1][2]));
    ac2[0][0] = MFMA16(wf[2][0], xf[4][0], ac2[0][0]);
    SF();
    wf[1][0] = wfrag64(L + 28672 + wa);
    SF();
    ac2[1][0] = MFMA16(wf[2][0], xf[4][1], ac2[1][0]);
    SF();
    wf[1][1] = wfrag64(L + 36864 + wa);
    SF();
    ac2[0][1] = MFMA16(wf[2][1], xf[4][0], ac2[0][1]);
    SF();
    wf[1][2] = wfrag64(L + 45056 + wa);
    SF();
    ac2[1][1] = MFMA16(wf[2][1], xf[4][1], ac2[1][1]);
    SF();
    ac2[0][2] = MFMA16(wf[2][2], xf[4][0], ac2[0][2]);
    SF();
    ac2[1][2] = MFMA16(wf[2][2], xf[4][1], ac2[1][2]);
    SF();
    asm volatile("" : "+v"(ac2[0][0]), "+v"(ac2[0][1]), "+v"(ac2[0][2]), "+v"(ac2[1][0]), "+v"(ac2[1][1]), "+v"(ac2[1][2]));
    ac2[0][0] = MFMA16(wf[0][0], xf[5][0], ac2[0][0]);
    SF();
    wf[2][0] = wfrag64(L + 30720 + wa);
    SF();
    ac2[1][0] = MFMA16(wf[0][0], xf[5][1], ac2[1][0]);
    SF();
    wf[2][1] = wfrag64(L + 38912 + wa);
    SF();
    ac2[0][1] = MFMA16(wf[0][1], xf[5][0], ac2[0][1]);
    SF();
    wf[2][2] = wfrag64(L + 47104 + wa);
    SF();
    ac2[1][1] = MFMA16(wf[0][1], xf[5][1], ac2[1][1]);
    SF();
    ac2[0][2] = MFMA16(wf[0][2], xf[5][0], ac2[0][2]);
    SF();
    ac2[1][2] = MFMA16(wf[0][2], xf[5][1], ac2[1][2]);
    SF();
    asm volatile("" : "+v"(ac2[0][0]), "+v"(ac2[0][1]), "+v"(ac2[0][2]), "+v"(ac2[1][0]), "+v"(ac2[1][1]), "+v"(ac2[1][2]));
    WAIT_VM(10);
    WAIT_VM(6);
    BARN(0);
    STAMP(11);
    ac2[0][0] = MFMA16(wf[1][0], xf[6][0], ac2[0][0]);
    SF();
    FLAG(WS_F0 + (rt * 16 + ct) * 128);
    wf[0][0] = wfrag64(L + 49152 + wa);
    SF();
    ac2[1][0] = MFMA16(wf[1][0], xf[6][1], ac2[1][0]);
    SF();
    wf[0][1] = wfrag64(L + 57344 + wa);
    SF();
    ac2[0][1] = MFMA16(wf[1][1], xf[6][0], ac2[0][1]);
    SF();
    wf[0][2] = wfrag64(L + 65536 + wa);
    SF();
    ac2[1][1] = MFMA16(wf[1][1], xf[6][1], ac2[1][1]);
    SF();
    ac2[0][2] = MFMA16(wf[1][2], xf[6][0], ac2[0][2]);
    SF();
    dW(1, 1, 0);
    SF();
    ac2[1][2] = MFMA16(wf[1][2], xf[6][1], ac2[1][2]);
    SF();
    dW(1, 1, 1);
    SF();
    asm volatile("" : "+v"(ac2[0][0]), "+v"(ac2[0][1]), "+v"(ac2[0][2]), "+v"(ac2[1][0]), "+v"(ac2[1][1]), "+v"(ac2[1][2]));
    ac2[0][0] = MFMA16(wf[2][0], xf[7][0], ac2[0][0]);
    SF();
    wf[1][0] = wfrag64(L + 51200 + wa);
    SF();
    ac2[1][0] = MFMA16(wf[2][0], xf[7][1], ac2[1][0]);
    SF();
    wf[1][1] = wfrag64(L + 59392 + wa);
    SF();
    ac2[0][1] = MFMA16(wf[2][1], xf[7][0], ac2[0][1]);
    SF();
    wf[1][2] = wfrag64(L + 67584 + wa);
    SF();
    ac2[1][1] = MFMA16(wf[2][1], xf[7][1], ac2[1][1]);
    SF();
    ac2[0][2] = MFMA16(wf[2][2], xf[7][0], ac2[0][2]);
    SF();
    dW(2, 2, 0);
    SF();
    ac2[1][2] = MFMA16(wf[2][2], xf[7][1], ac2[1][2]);
    SF();
    dW(2, 2, 1);
    SF();
    asm volatile("" : "+v"(ac2[0][0]), "+v"(ac2[0][1]), "+v"(ac2[0][2]), "+v"(ac2[1][0]), "+v"(ac2[1][1]), "+v"(ac2[1][2]));
    ac2[0][0] = MFMA16(wf[0][0], xf[8][0], ac2[0][0]);
    SF();
    wf[2][0] = wfrag64(L + 53248 + wa);
    SF();
    ac2[1][0] = MFMA16(wf[0][0], xf[8][1], ac2[1][0]);
    SF();
    wf[2][1] = wfrag64(L + 61440 + wa);
    SF();
    ac2[0][1] = MFMA16(wf[0][1], xf[8][0], ac2[0][1]);
    SF();
    wf[2][2] = wfrag64(L + 69632 + wa);
    SF();
    ac2[1][1] = MFMA16(wf[0][1], xf[8][1], ac2[1][1]);
    SF();
    ac2[0][2] = MFMA16(wf[0][2], xf[8][0], ac2[0][2]);
    SF();
    ac2[1][2] = MFMA16(wf[0][2], xf[8][1], ac2[1][2]);
    SF();
    asm volatile("" : "+v"(ac2[0][0]), "+v"(ac2[0][1]), "+v"(ac2[0][2]), "+v"(ac2[1][0]), "+v"(ac2[1][1]), "+v"(ac2[1][2]));
    ac2[0][0] = MFMA16(wf[1][0], xf[9][0], ac2[0][0]);
    SF();
    wf[0][0] = wfrag64(L + 55296 + wa);
    SF();
    ac2[1][0] = MFMA16(wf[1][0], xf[9][1], ac2[1][0]);
    SF();
    wf[0][1] = wfrag64(L + 63488 + wa);
    SF();
    ac2[0][1] = MFMA16(wf[1][1], xf[9][0], ac2[0][1]);
    SF();
    wf[0][2] = wfrag64(L + 71680 + wa);
    SF();
    ac2[1][1] = MFMA16(wf[1][1], xf[9][1], ac2[1][1]);
    SF();
    ac2[0][2] = MFMA16(wf[1][2], xf[9][0], ac2[0][2]);
    SF();
    ac2[1][2] = MFMA16(wf[1][2], xf[9][1], ac2[1][2]);
    SF();
    asm volatile("" : "+v"(ac2[0][0]), "+v"(ac2[0][1]), "+v"(ac2[0][2]), "+v"(ac2[1][0]), "+v"(ac2[1][1]), "+v"(ac2[1][2]));
    WAIT_VM(7);
    BARN(2);
    STAMP(12);
    ac2[0][0] = MFMA16(wf[2][0], xf[10][0], ac2[0][0]);
    SF();
    wf[1][0] = wfrag64(L + 73728 + wa);
    SF();
    ac2[1][0] = MFMA16(wf[2][0], xf[10][1], ac2[1][0]);
    SF();
    wf[1][1] = wfrag64(L + 81920 + wa);
    SF();
    ac2[0][1] = MFMA16(wf[2][1], xf[10][0], ac2[0][1]);
    SF();
    wf[1][2] = wfrag64(L + 90112 + wa);
    SF();
    ac2[1][1] = MFMA16(wf[2][1], xf[10][1], ac2[1][1]);
    SF();
    ac2[0][2] = MFMA16(wf[2][2], xf[10][0], ac2[0][2]);
    SF();
    dW(3, 3, 0);
    SF();
    ac2[1][2] = MFMA16(wf[2][2], xf[10][1], ac2[1][2]);
    SF();
    dW(3, 3, 1);
    fl = ld8c(ws, f0off);
    SF();
    asm volatile("" : "+v"(ac2[0][0]), "+v"(ac2[0][1]), "+v"(ac2[0][2]), "+v"(ac2[1][0]), "+v"(ac2[1][1]), "+v"(ac2[1][2]));
    ac2[0][0] = MFMA16(wf[0][0], xf[11][0], ac2[0][0]);
    SF();
    wf[2][0] = wfrag64(L + 75776 + wa);
    SF();
    ac2[1][0] = MFMA16(wf[0][0], xf[11][1], ac2[1][0]);
    SF();
    wf[2][1] = wfrag64(L + 83968 + wa);
    SF();
    ac2[0][1] = MFMA16(wf[0][1], xf[11][0], ac2[0][1]);
    SF();
    wf[2][2] = wfrag64(L + 92160 + wa);
    SF();
    ac2[1][1] = MFMA16(wf[0][1], xf[11][1], ac2[1][1]);
    SF();
    ac2[0][2] = MFMA16(wf[0][2], xf[11][0], ac2[0][2]);
    SF();
    ac2[1][2] = MFMA16(wf[0][2], xf[11][1], ac2[1][2]);
    SF();
    asm volatile("" : "+v"(ac2[0][0]), "+v"(ac2[0][1]), "+v"(ac2[0][2]), "+v"(ac2[1][0]), "+v"(ac2[1][1]), "+v"(ac2[1][2]));
    ac2[0][0] = MFMA16(wf[1][0], xf[12][0], ac2[0][0]);
    SF();
    wf[0][0] = wfrag64(L + 77824 + wa);
    SF();
    ac2[1][0] = MFMA16(wf[1][0], xf[12][1], ac2[1][0]);
    SF();
    wf[0][1] = wfrag64(L + 86016 + wa);
    SF();
    ac2[0][1] = MFMA16(wf[1][1], xf[12][0], ac2[0][1]);
    SF();
    wf[0][2] = wfrag64(L + 94208 + wa);
    SF();
    ac2[1][1] = MFMA16(wf[1][1], xf[12][1], ac2[1][1]);
    SF();
    ac2[0][2] = MFMA16(wf[1][2], xf[12][0], ac2[0][2]);
    SF();
    ac2[1][2] = MFMA16(wf[1][2], xf[12][1], ac2[1][2]);
    SF();
    asm volatile("" : "+v"(ac2[0][0]), "+v"(ac2[0][1]), "+v"(ac2[0][2]), "+v"(ac2[1][0]), "+v"(ac2[1][1]), "+v"(ac2[1][2]));
    WAIT_VM(0);
    PIN(fl);
    spin_flags(ws, f0off, fl, tag);
    STAMP(13);
    {
        const char* src = ws + WS_KV + (size_t)(((ct & 14) + ((wid >> 1) & 1)) * 16384 + (((ct & 1) * 2 + (wid >> 2)) * 4 + (wid & 1) * 2) * 1024);
        const unsigned dst = lds0 + (unsigned)((wid >> 2) * 24576 + ((wid >> 1) & 1) * 12288 + (wid & 1) * 2048);
        glds16c(src + 0, (unsigned)(lane * 16 + b2 * 262144), dst, XR + 8192);
        glds16c(src + 1024, (unsigned)(lane * 16 + b2 * 262144), dst, XR + 9216);
        glds16c(src + 0, (unsigned)(lane * 16 + b0 * 262144), dst, XR + 0);
        glds16c(src + 1024, (unsigned)(lane * 16 + b0 * 262144), dst, XR + 1024);
        glds16c(src + 0, (unsigned)(lane * 16 + b1 * 262144), dst, XR + 4096);
        glds16c(src + 1024, (unsigned)(lane * 16 + b1 * 262144), dst, XR + 5120);
    }
    SF();
    ac2[0][0] = MFMA16(wf[2][0], xf[13][0], ac2[0][0]);
    SF();
    wf[1][0] = wfrag64(L + 79872 + wa);
    SF();
    ac2[1][0] = MFMA16(wf[2][0], xf[13][1], ac2[1][0]);
    SF();
    wf[1][1] = wfrag64(L + 88064 + wa);
    SF();
    ac2[0][1] = MFMA16(wf[2][1], xf[13][0], ac2[0][1]);
    SF();
    wf[1][2] = wfrag64(L + 96256 + wa);
    SF();
    ac2[1][1] = MFMA16(wf[2][1], xf[13][1], ac2[1][1]);
    SF();
    ac2[0][2] = MFMA16(wf[2][2], xf[13][0], ac2[0][2]);
    SF();
    ac2[1][2] = MFMA16(wf[2][2], xf[13][1], ac2[1][2]);
    SF();
    asm volatile("" : "+v"(ac2[0][0]), "+v"(ac2[0][1]), "+v"(ac2[0][2]), "+v"(ac2[1][0]), "+v"(ac2[1][1]), "+v"(ac2[1][2]));
    ac2[0][0] = MFMA16(wf[0][0], xf[14][0], ac2[0][0]);
    SF();
    ac2[1][0] = MFMA16(wf[0][0], xf[14][1], ac2[1][0]);
    SF();
    ac2[0][1] = MFMA16(wf[0][1], xf[14][0], ac2[0][1]);
    SF();
    ac2[1][1] = MFMA16(wf[0][1], xf[14][1], ac2[1][1]);
    SF();
    ac2[0][2] = MFMA16(wf[0][2], xf[14][0], ac2[0][2]);
    SF();
    ac2[1][2] = MFMA16(wf[0][2], xf[14][1], ac2[1][2]);
    SF();
    asm volatile("" : "+v"(ac2[0][0]), "+v"(ac2[0][1]), "+v"(ac2[0][2]), "+v"(ac2[1][0]), "+v"(ac2[1][1]), "+v"(ac2[1][2]));
    ac2[0][0] = MFMA16(wf[1][0], xf[15][0], ac2[0][0]);
    SF();
    ac2[1][0] = MFMA16(wf[1][0], xf[15][1], ac2[1][0]);
    SF();
    ac2[0][1] = MFMA16(wf[1][1], xf[15][0], ac2[0][1]);
    SF();
    ac2[1][1] = MFMA16(wf[1][1], xf[15][1], ac2[1][1]);
    SF();
    ac2[0][2] = MFMA16(wf[1][2], xf[15][0], ac2[0][2]);
    SF();
    ac2[1][2] = MFMA16(wf[1][2], xf[15][1], ac2[1][2]);
    SF();
    asm volatile("" : "+v"(ac2[0][0]), "+v"(ac2[0][1]), "+v"(ac2[0][2]), "+v"(ac2[1][0]), "+v"(ac2[1][1]), "+v"(ac2[1][2]));
    STAMP(14);
    ST2(0);
    ST2(1);
    ST2(2);
    SF();

    unsigned pmask[8];
#pragma unroll
    for (int i = 0; i < 8; ++i) {
        const int b0 = ((2 * i) & 3) + 8 * ((2 * i) >> 2);
        pmask[i] = (b0 > aa ? 0xffffu : 0u) | (b0 + 1 > aa ? 0xffff0000u : 0u);
    }

    WAIT_VM(3);
    BAR();
    STAMP(15);
    dW(4, 4, 0);
    dW(4, 4, 1);
    dW(5, 5, 0);
    dW(5, 5, 1);
    SF();
    WAIT_VM(4);
    BAR();
    FLAG(WS_F1 + (rt * 16 + ct) * 128);
    SF();
    STAMP(16);

    const bf16x8 qf = asbf(qraw);
    const lds_cptr Kl = L + XR + wc * 24576 + wm * 1024 + lane * 16;
    const int i16 = lane & 15, grp = (lane >> 4) & 1;
    const unsigned vbase = grp ? (unsigned)159872 : (unsigned)(XR + wc * 24576 + 12288 + wm * 1024 + ((i16 >> 1) & 1) * 512 + ((4 * (h ^ ((i16 >> 1) & 1)) + (i16 >> 2)) << 4) + (i16 & 1) * 8);
    const unsigned vstep = grp ? 0u : 1024u;
    const int tp = wid & 1, kq = wid >> 1;
    const unsigned xl = (unsigned)((g >> 1) * 512 + fr * 32 + (g & 1) * 16);
    const char* Zb[2] = {ws + WS_Z + (size_t)((mt * 4 + 2 * tp + (kq >> 1)) * KB_Z + 2 * kq) * 512, ws + WS_Z + (size_t)((mt * 4 + 2 * tp + 1 - (kq >> 1)) * KB_Z + 2 * kq) * 512};
    const int x10 = kq == 3 ? 78 * 512 : 80 * 512;
    u32x4 xr[15][2], gl[2];
#define XL(s, a) xr[s][a] = XLD(Zb[a] + ((s) == 10 ? x10 : ((s) < 11 ? 8 * PH(s) : KB_H + 8 * PH(s)) * 512), xl)
    const float NEG = -__builtin_inff();
    f32x16 zero;
#pragma unroll
    for (int r = 0; r < 16; ++r) zero[r] = 0.f;
    const bf16x8 k8 = ldsr16(Kl + 8 * 1024), k0 = ldsr16(Kl);
    bf16x8 kf[2];
    kf[1] = ldsr16(Kl + 7 * 1024);
    const f32x16 sd = MFMA32(k8, qf, zero);
    float mx = fmaxf(fmaxf(fmaxf(fmaxf(sd[0], sd[1]), sd[2]), fmaxf(fmaxf(sd[3], sd[4]), sd[5])), fmaxf(fmaxf(fmaxf(sd[6], sd[7]), sd[8]), fmaxf(fmaxf(sd[9], sd[10]), sd[11])));
    mx = fmaxf(fmaxf(fmaxf(mx, sd[12]), sd[13]), fmaxf(sd[14], sd[15]));
    {
        auto rr = __builtin_amdgcn_permlane32_swap(__float_as_uint(mx), __float_as_uint(mx), false, false);
        mx = fmaxf(__uint_as_float(rr[0]), __uint_as_float(rr[1]));
    }
    f32x16 nm;
#pragma unroll
    for (int r = 0; r < 16; ++r) nm[r] = -mx;
    f32x16 o = zero;
    auto pv = [&](int t, const unsigned* pw) {
        const lds_cptr vp = L + (vbase + vstep * (unsigned)t);
#pragma unroll
        for (int ss = 0; ss < 2; ++ss) {
            const u32x4 w = {pw[4 * ss], pw[4 * ss + 1], pw[4 * ss + 2], pw[4 * ss + 3]};
            const bf16x8 vf = cat8(vtr(vp + ss * 256), vtr(vp + ss * 256 + 128));
            o = MFMA32(vf, __builtin_bit_cast(bf16x8, w), o);
        }
    };
    f32x16 sn;
    {
        f32x16 s8 = qk(k8, qf, nm), s0 = qk(k0, qf, nm);
        sn = qk(kf[1], qf, nm);
        kf[0] = ldsr16(Kl + 6 * 1024);
        asm volatile("s_nop 7\n\ts_nop 3" : "+v"(s8), "+v"(s0));
        unsigned pw[8], p0_[8], p8[8];
#pragma unroll
        for (int i = 0; i < 8; ++i) {
            const int b = ((2 * i) & 3) + 8 * ((2 * i) >> 2);
            pw[i] = cvtpk(__builtin_amdgcn_exp2f(b > aa ? s0[2 * i] : s8[2 * i]), __builtin_amdgcn_exp2f(b + 1 > aa ? s0[2 * i + 1] : s8[2 * i + 1]));
            p0_[i] = pw[i] & pmask[i];
            p8[i] = pw[i] ^ p0_[i];
        }
        pv(8, p8);
        if (__builtin_expect(!no0, 1)) pv(0, p0_);
    }
    {
        const f32x16 sc = sn;
        SF();
        kf[1] = ldsr16(Kl + 5120);
        sn = qk(kf[0], qf, nm);
        const lds_cptr vp = L + (vbase + vstep * 7u);
        const bf16x8 v0 = cat8(vtr(vp), vtr(vp + 128)), v1 = cat8(vtr(vp + 256), vtr(vp + 384));
        SF();
        if (__builtin_expect(t0 <= 7, 1)) {
        u32x4 pa, pb;
        #pragma unroll
        for (int i = 0; i < 4; ++i) pa[i] = cvtpk(__builtin_amdgcn_exp2f(sc[2 * i]), __builtin_amdgcn_exp2f(sc[2 * i + 1]));
        o = MFMA32(v0, asbf(pa), o);
        #pragma unroll
        for (int i = 0; i < 4; ++i) pb[i] = cvtpk(__builtin_amdgcn_exp2f(sc[8 + 2 * i]), __builtin_amdgcn_exp2f(sc[9 + 2 * i]));
        o = MFMA32(v1, asbf(pb), o);
        }
        SF();
    }
    fl = ld8c(ws, f1off);
    {
        const f32x16 sc = sn;
        SF();
        kf[0] = ldsr16(Kl + 4096);
        sn = qk(kf[1], qf, nm);
        const lds_cptr vp = L + (vbase + vstep * 6u);
        const bf16x8 v0 = cat8(vtr(vp), vtr(vp + 128)), v1 = cat8(vtr(vp + 256), vtr(vp + 384));
        SF();
        if (__builtin_expect(t0 <= 6, 1)) {
        u32x4 pa, pb;
        #pragma unroll
        for (int i = 0; i < 4; ++i) pa[i] = cvtpk(__builtin_amdgcn_exp2f(sc[2 * i]), __builtin_amdgcn_exp2f(sc[2 * i + 1]));
        o = MFMA32(v0, asbf(pa), o);
        #pragma unroll
        for (int i = 0; i < 4; ++i) pb[i] = cvtpk(__builtin_amdgcn_exp2f(sc[8 + 2 * i]), __builtin_amdgcn_exp2f(sc[9 + 2 * i]));
        o = MFMA32(v1, asbf(pb), o);
        }
        SF();
    }
    SF();
    WAIT_VM(0);
    PIN(fl);
    spin_flags(ws, f1off, fl, tag);
    STAMP(17);
    gl[0] = ld16c(ws + WS_G, (unsigned)(g * 32768 + (mt * 64 + tp * 32 + (kq >> 1) * 16 + fr) * 16));
    gl[1] = ld16c(ws + WS_G, (unsigned)(g * 32768 + (mt * 64 + tp * 32 + (1 - (kq >> 1)) * 16 + fr) * 16));
    SF();
    XL(10, 0);
    XL(10, 1);
    XL(0, 0);
    SF();
    {
        const f32x16 sc = sn;
        SF();
        kf[1] = ldsr16(Kl + 3072);
        sn = qk(kf[0], qf, nm);
        const lds_cptr vp = L + (vbase + vstep * 5u);
        const bf16x8 v0 = cat8(vtr(vp), vtr(vp + 128)), v1 = cat8(vtr(vp + 256), vtr(vp + 384));
        SF();
        if (__builtin_expect(t0 <= 5, 1)) {
        u32x4 pa, pb;
        #pragma unroll
        for (int i = 0; i < 4; ++i) pa[i] = cvtpk(__builtin_amdgcn_exp2f(sc[2 * i]), __builtin_amdgcn_exp2f(sc[2 * i + 1]));
        o = MFMA32(v0, asbf(pa), o);
        #pragma unroll
        for (int i = 0; i < 4; ++i) pb[i] = cvtpk(__builtin_amdgcn_exp2f(sc[8 + 2 * i]), __builtin_amdgcn_exp2f(sc[9 + 2 * i]));
        o = MFMA32(v1, asbf(pb), o);
        }
        SF();
    }
    SF();
    XL(0, 1);
    XL(1, 0);
    XL(1, 1);
    SF();
    {
        const f32x16 sc = sn;
        SF();
        kf[0] = ldsr16(Kl + 2048);
        sn = qk(kf[1], qf, nm);
        const lds_cptr vp = L + (vbase + vstep * 4u);
        const bf16x8 v0 = cat8(vtr(vp), vtr(vp + 128)), v1 = cat8(vtr(vp + 256), vtr(vp + 384));
        SF();
        if (__builtin_expect(t0 <= 4, 1)) {
        u32x4 pa, pb;
        #pragma unroll
        for (int i = 0; i < 4; ++i) pa[i] = cvtpk(__builtin_amdgcn_exp2f(sc[2 * i]), __builtin_amdgcn_exp2f(sc[2 * i + 1]));
        o = MFMA32(v0, asbf(pa), o);
        #pragma unroll
        for (int i = 0; i < 4; ++i) pb[i] = cvtpk(__builtin_amdgcn_exp2f(sc[8 + 2 * i]), __builtin_amdgcn_exp2f(sc[9 + 2 * i]));
        o = MFMA32(v1, asbf(pb), o);
        }
        SF();
    }
    SF();
    XL(2, 0);
    XL(2, 1);
    XL(3, 0);
    SF();
    {
        const f32x16 sc = sn;
        SF();
        kf[1] = ldsr16(Kl + 1024);
        sn = qk(kf[0], qf, nm);
        const lds_cptr vp = L + (vbase + vstep * 3u);
        const bf16x8 v0 = cat8(vtr(vp), vtr(vp + 128)), v1 = cat8(vtr(vp + 256), vtr(vp + 384));
        SF();
        if (__builtin_expect(t0 <= 3, 1)) {
        u32x4 pa, pb;
        #pragma unroll
        for (int i = 0; i < 4; ++i) pa[i] = cvtpk(__builtin_amdgcn_exp2f(sc[2 * i]), __builtin_amdgcn_exp2f(sc[2 * i + 1]));
        o = MFMA32(v0, asbf(pa), o);
        #pragma unroll
        for (int i = 0; i < 4; ++i) pb[i] = cvtpk(__builtin_amdgcn_exp2f(sc[8 + 2 * i]), __builtin_amdgcn_exp2f(sc[9 + 2 * i]));
        o = MFMA32(v1, asbf(pb), o);
        }
        SF();
    }
    SF();
    XL(3, 1);
    XL(4, 0);
    XL(4, 1);
    SF();
    {
        const f32x16 sc = sn;
        SF();
        sn = qk(kf[1], qf, nm);
        const lds_cptr vp = L + (vbase + vstep * 2u);
        const bf16x8 v0 = cat8(vtr(vp), vtr(vp + 128)), v1 = cat8(vtr(vp + 256), vtr(vp + 384));
        SF();
        if (__builtin_expect(t0 <= 2, 1)) {
        u32x4 pa, pb;
        #pragma unroll
        for (int i = 0; i < 4; ++i) pa[i] = cvtpk(__builtin_amdgcn_exp2f(sc[2 * i]), __builtin_amdgcn_exp2f(sc[2 * i + 1]));
        o = MFMA32(v0, asbf(pa), o);
        #pragma unroll
        for (int i = 0; i < 4; ++i) pb[i] = cvtpk(__builtin_amdgcn_exp2f(sc[8 + 2 * i]), __builtin_amdgcn_exp2f(sc[9 + 2 * i]));
        o = MFMA32(v1, asbf(pb), o);
        }
        SF();
    }
    SF();
    XL(5, 0);
    XL(5, 1);
    XL(6, 0);
    SF();
    {
        const f32x16 sc = sn;
        SF();
        const lds_cptr vp = L + (vbase + vstep * 1u);
        const bf16x8 v0 = cat8(vtr(vp), vtr(vp + 128)), v1 = cat8(vtr(vp + 256), vtr(vp + 384));
        SF();
        if (__builtin_expect(t0 <= 1, 1)) {
        u32x4 pa, pb;
        #pragma unroll
        for (int i = 0; i < 4; ++i) pa[i] = cvtpk(__builtin_amdgcn_exp2f(sc[2 * i]), __builtin_amdgcn_exp2f(sc[2 * i + 1]));
        o = MFMA32(v0, asbf(pa), o);
        #pragma unroll
        for (int i = 0; i < 4; ++i) pb[i] = cvtpk(__builtin_amdgcn_exp2f(sc[8 + 2 * i]), __builtin_amdgcn_exp2f(sc[9 + 2 * i]));
        o = MFMA32(v1, asbf(pb), o);
        }
        SF();
    }
    SF();
    XL(6, 1);
    XL(7, 0);
    XL(7, 1);
    SF();
    STAMP(18);
    if (__builtin_expect(__any(!(o[8] < 1.0e9f && o[8] > 1.0e-18f)), 0)) {
        float m2 = NEG;
#pragma unroll 1
        for (int ps = 0; ps < 2; ++ps) {
            o = zero;
#pragma unroll 1
            for (int t = t0; t <= 8; ++t) {
                const f32x16 s = MFMA32(ldsr16(Kl + t * 1024), qf, zero);
                const int lo = t == 0 ? aa + 1 : -64, hi = t == 8 ? aa : 64;
                unsigned pw[8];
                float mt_ = NEG;
#pragma unroll
                for (int i = 0; i < 8; ++i) {
                    const int b = ((2 * i) & 3) + 8 * ((2 * i) >> 2);
                    const float s0 = (b >= lo && b <= hi) ? s[2 * i] : NEG, s1 = (b + 1 >= lo && b + 1 <= hi) ? s[2 * i + 1] : NEG;
                    mt_ = fmaxf(mt_, fmaxf(s0, s1));
                    pw[i] = cvtpk(__builtin_amdgcn_exp2f(s0 - m2), __builtin_amdgcn_exp2f(s1 - m2));
                }
                if (ps == 0) mx = t == t0 ? mt_ : fmaxf(mx, mt_);
                else pv(t, pw);
            }
            auto rr = __builtin_amdgcn_permlane32_swap(__float_as_uint(mx), __float_as_uint(mx), false, false);
            m2 = fmaxf(__uint_as_float(rr[0]), __uint_as_float(rr[1]));
        }
    }
    {
        const float inv = 1.0f / o[8];
        const u32x4 ow = {cvtpk(o[0] * inv, o[1] * inv), cvtpk(o[2] * inv, o[3] * inv), cvtpk(o[4] * inv, o[5] * inv), cvtpk(o[6] * inv, o[7] * inv)};
        const int tok = rt * 128 + wm * 32 + r32;
        __builtin_amdgcn_raw_buffer_store_b128(ow, wsr, WS_Z + (unsigned)(((tok >> 4) * KB_Z + KB_H + 2 * ct + wc) * 512 + (tok & 15) * 32 + h * 16), 0, 16);
    }

    const int fx = (q4 >> 1) | ((g & 1) << 1);
    const unsigned wrow = (unsigned)(kq * 4096 + (g * 8 + q4) * 128 + p4 * 8);
    unsigned pa[4];
#pragma unroll
    for (int b = 0; b < 4; ++b) pa[b] = wrow + (unsigned)((((((kq & 1) ^ (b >> 1)) << 1) | (b & 1)) ^ fx) << 5);
    f32x4 ca[2][4], cb[2][4];
#pragma unroll
    for (int a = 0; a < 2; ++a)
#pragma unroll
        for (int b = 0; b < 4; ++b) { ca[a][b] = (f32x4){0.f, 0.f, 0.f, 0.f}; cb[a][b] = ca[a][b]; }
    bf16x8 pw3[2][4];
    float gg[2];
    u64 fe[4];

    pw3[0][0] = wfrag(L + 147456 + pa[0]);
    pw3[0][1] = wfrag(L + 147456 + pa[1]);
    pw3[0][2] = wfrag(L + 147456 + pa[2]);
    pw3[0][3] = wfrag(L + 147456 + pa[3]);
    SF();
    WAIT_VM(17);
    PIN(xr[10][0]); PIN(xr[10][1]);
    if (kq != 3) {
        cb[0][0] = MFMA16(pw3[0][0], asbf(xr[10][0]), cb[0][0]);
        cb[1][0] = MFMA16(pw3[0][0], asbf(xr[10][1]), cb[1][0]);
        cb[0][1] = MFMA16(pw3[0][1], asbf(xr[10][0]), cb[0][1]);
        cb[1][1] = MFMA16(pw3[0][1], asbf(xr[10][1]), cb[1][1]);
        cb[0][2] = MFMA16(pw3[0][2], asbf(xr[10][0]), cb[0][2]);
        cb[1][2] = MFMA16(pw3[0][2], asbf(xr[10][1]), cb[1][2]);
        cb[0][3] = MFMA16(pw3[0][3], asbf(xr[10][0]), cb[0][3]);
        cb[1][3] = MFMA16(pw3[0][3], asbf(xr[10][1]), cb[1][3]);
    }
    SF();
    pw3[1][0] = wfrag(L + 0 + pa[0]);
    pw3[1][1] = wfrag(L + 0 + pa[1]);
    pw3[1][2] = wfrag(L + 0 + pa[2]);
    pw3[1][3] = wfrag(L + 0 + pa[3]);
    XL(8, 0);
    XL(8, 1);
    SF();
    BAR();
    STAMP(19);
    WAIT_VM(17);
    PIN(xr[0][0]); PIN(xr[0][1]);
    cb[0][0] = MFMA16(pw3[1][0], asbf(xr[0][0]), cb[0][0]);
    SF();
    pw3[0][0] = wfrag(L + 16384 + pa[0]);
    SF();
    cb[1][0] = MFMA16(pw3[1][0], asbf(xr[0][1]), cb[1][0]);
    SF();
    pw3[0][1] = wfrag(L + 16384 + pa[1]);
    SF();
    cb[0][1] = MFMA16(pw3[1][1], asbf(xr[0][0]), cb[0][1]);
    SF();
    pw3[0][2] = wfrag(L + 16384 + pa[2]);
    SF();
    cb[1][1] = MFMA16(pw3[1][1], asbf(xr[0][1]), cb[1][1]);
    SF();
    pw3[0][3] = wfrag(L + 16384 + pa[3]);
    SF();
    cb[0][2] = MFMA16(pw3[1][2], asbf(xr[0][0]), cb[0][2]);
    SF();
    XL(9, 0);
    SF();
    cb[1][2] = MFMA16(pw3[1][2], asbf(xr[0][1]), cb[1][2]);
    SF();
    XL(9, 1);
    SF();
    cb[0][3] = MFMA16(pw3[1][3], asbf(xr[0][0]), cb[0][3]);
    SF();
    dW(6, 6, 0);
    SF();
    cb[1][3] = MFMA16(pw3[1][3], asbf(xr[0][1]), cb[1][3]);
    SF();
    dW(6, 6, 1);
    SF();
    WAIT_VM(19);
    PIN(xr[1][0]); PIN(xr[1][1]);
    PIN(gl[0]); PIN(gl[1]);
    #pragma unroll
    for (int a = 0; a < 2; ++a) {
        const float lg = sum_rows((__uint_as_float(gl[a][0]) + __uint_as_float(gl[a][1])) + (__uint_as_float(gl[a][2]) + __uint_as_float(gl[a][3])));
        gg[a] = 1.0f / (1.0f + __expf(-lg));
    }
    cb[0][0] = MFMA16(pw3[0][0], asbf(xr[1][0]), cb[0][0]);
    SF();
    pw3[1][0] = wfrag(L + 32768 + pa[0]);
    SF();
    cb[1][0] = MFMA16(pw3[0][0], asbf(xr[1][1]), cb[1][0]);
    SF();
    pw3[1][1] = wfrag(L + 32768 + pa[1]);
    SF();
    cb[0][1] = MFMA16(pw3[0][1], asbf(xr[1][0]), cb[0][1]);
    SF();
    pw3[1][2] = wfrag(L + 32768 + pa[2]);
    SF();
    cb[1][1] = MFMA16(pw3[0][1], asbf(xr[1][1]), cb[1][1]);
    SF();
    pw3[1][3] = wfrag(L + 32768 + pa[3]);
    SF();
    cb[0][2] = MFMA16(pw3[0][2], asbf(xr[1][0]), cb[0][2]);
    SF();
    dW(7, 7, 0);
    SF();
    cb[1][2] = MFMA16(pw3[0][2], asbf(xr[1][1]), cb[1][2]);
    SF();
    dW(7, 7, 1);
    SF();
    cb[0][3] = MFMA16(pw3[0][3], asbf(xr[1][0]), cb[0][3]);
    SF();
    dW(8, 8, 0);
    SF();
    cb[1][3] = MFMA16(pw3[0][3], asbf(xr[1][1]), cb[1][3]);
    SF();
    dW(8, 8, 1);
    SF();
    WAIT_VM(21);
    PIN(xr[2][0]); PIN(xr[2][1]);
    cb[0][0] = MFMA16(pw3[1][0], asbf(xr[2][0]), cb[0][0]);
    SF();
    pw3[0][0] = wfrag(L + 49152 + pa[0]);
    SF();
    cb[1][0] = MFMA16(pw3[1][0], asbf(xr[2][1]), cb[1][0]);
    SF();
    pw3[0][1] = wfrag(L + 49152 + pa[1]);
    SF();
    cb[0][1] = MFMA16(pw3[1][1], asbf(xr[2][0]), cb[0][1]);
    SF();
    pw3[0][2] = wfrag(L + 49152 + pa[2]);
    SF();
    cb[1][1] = MFMA16(pw3[1][1], asbf(xr[2][1]), cb[1][1]);
    SF();
    pw3[0][3] = wfrag(L + 49152 + pa[3]);
    SF();
    cb[0][2] = MFMA16(pw3[1][2], asbf(xr[2][0]), cb[0][2]);
    SF();
    dW(9, 9, 0);
    SF();
    cb[1][2] = MFMA16(pw3[1][2], asbf(xr[2][1]), cb[1][2]);
    SF();
    dW(9, 9, 1);
    SF();
    cb[0][3] = MFMA16(pw3[1][3], asbf(xr[2][0]), cb[0][3]);
    SF();
    dW(11, 0, 0);
    SF();
    cb[1][3] = MFMA16(pw3[1][3], asbf(xr[2][1]), cb[1][3]);
    SF();
    dW(11, 0, 1);
    SF();
    WAIT_VM(14);
    BAR();
    STAMP(20);
    FLAG(WS_F3 + (rt * 16 + ct) * 128);
    if (blockIdx.x == 0 && wid == 7) {
    #pragma unroll
        for (int i = 0; i < 4; ++i) fe[i] = ld8c(ws, WS_F1 + (unsigned)(lane + 64 * i) * 128);
    }
    PIN(xr[3][0]); PIN(xr[3][1]);
    cb[0][0] = MFMA16(pw3[0][0], asbf(xr[3][0]), cb[0][0]);
    SF();
    pw3[1][0] = wfrag(L + 65536 + pa[0]);
    SF();
    cb[1][0] = MFMA16(pw3[0][0], asbf(xr[3][1]), cb[1][0]);
    SF();
    pw3[1][1] = wfrag(L + 65536 + pa[1]);
    SF();
    cb[0][1] = MFMA16(pw3[0][1], asbf(xr[3][0]), cb[0][1]);
    SF();
    pw3[1][2] = wfrag(L + 65536 + pa[2]);
    SF();
    cb[1][1] = MFMA16(pw3[0][1], asbf(xr[3][1]), cb[1][1]);
    SF();
    pw3[1][3] = wfrag(L + 65536 + pa[3]);
    SF();
    cb[0][2] = MFMA16(pw3[0][2], asbf(xr[3][0]), cb[0][2]);
    SF();
    cb[1][2] = MFMA16(pw3[0][2], asbf(xr[3][1]), cb[1][2]);
    SF();
    cb[0][3] = MFMA16(pw3[0][3], asbf(xr[3][0]), cb[0][3]);
    SF();
    cb[1][3] = MFMA16(pw3[0][3], asbf(xr[3][1]), cb[1][3]);
    SF();
    PIN(xr[4][0]); PIN(xr[4][1]);
    cb[0][0] = MFMA16(pw3[1][0], asbf(xr[4][0]), cb[0][0]);
    SF();
    pw3[0][0] = wfrag(L + 81920 + pa[0]);
    SF();
    cb[1][0] = MFMA16(pw3[1][0], asbf(xr[4][1]), cb[1][0]);
    SF();
    pw3[0][1] = wfrag(L + 81920 + pa[1]);
    SF();
    cb[0][1] = MFMA16(pw3[1][1], asbf(xr[4][0]), cb[0][1]);
    SF();
    pw3[0][2] = wfrag(L + 81920 + pa[2]);
    SF();
    cb[1][1] = MFMA16(pw3[1][1], asbf(xr[4][1]), cb[1][1]);
    SF();
    pw3[0][3] = wfrag(L + 81920 + pa[3]);
    SF();
    cb[0][2] = MFMA16(pw3[1][2], asbf(xr[4][0]), cb[0][2]);
    SF();
    dW(12, 1, 0);
    SF();
    cb[1][2] = MFMA16(pw3[1][2], asbf(xr[4][1]), cb[1][2]);
    SF();
    dW(12, 1, 1);
    SF();
    cb[0][3] = MFMA16(pw3[1][3], asbf(xr[4][0]), cb[0][3]);
    SF();
    dW(13, 2, 0);
    SF();
    cb[1][3] = MFMA16(pw3[1][3], asbf(xr[4][1]), cb[1][3]);
    SF();
    dW(13, 2, 1);
    SF();
    WAIT_VM(7);
    BAR();
    STAMP(21);
    PIN(xr[5][0]); PIN(xr[5][1]);
    cb[0][0] = MFMA16(pw3[0][0], asbf(xr[5][0]), cb[0][0]);
    SF();
    pw3[1][0] = wfrag(L + 98304 + pa[0]);
    SF();
    cb[1][0] = MFMA16(pw3[0][0], asbf(xr[5][1]), cb[1][0]);
    SF();
    pw3[1][1] = wfrag(L + 98304 + pa[1]);
    SF();
    cb[0][1] = MFMA16(pw3[0][1], asbf(xr[5][0]), cb[0][1]);
    SF();
    pw3[1][2] = wfrag(L + 98304 + pa[2]);
    SF();
    cb[1][1] = MFMA16(pw3[0][1], asbf(xr[5][1]), cb[1][1]);
    SF();
    pw3[1][3] = wfrag(L + 98304 + pa[3]);
    SF();
    cb[0][2] = MFMA16(pw3[0][2], asbf(xr[5][0]), cb[0][2]);
    SF();
    dW(14, 3, 0);
    SF();
    cb[1][2] = MFMA16(pw3[0][2], asbf(xr[5][1]), cb[1][2]);
    SF();
    dW(14, 3, 1);
    SF();
    cb[0][3] = MFMA16(pw3[0][3], asbf(xr[5][0]), cb[0][3]);
    SF();
    cb[1][3] = MFMA16(pw3[0][3], asbf(xr[5][1]), cb[1][3]);
    SF();
    PIN(xr[6][0]); PIN(xr[6][1]);
    cb[0][0] = MFMA16(pw3[1][0], asbf(xr[6][0]), cb[0][0]);
    SF();
    pw3[0][0] = wfrag(L + 114688 + pa[0]);
    SF();
    cb[1][0] = MFMA16(pw3[1][0], asbf(xr[6][1]), cb[1][0]);
    SF();
    pw3[0][1] = wfrag(L + 114688 + pa[1]);
    SF();
    cb[0][1] = MFMA16(pw3[1][1], asbf(xr[6][0]), cb[0][1]);
    SF();
    pw3[0][2] = wfrag(L + 114688 + pa[2]);
    SF();
    cb[1][1] = MFMA16(pw3[1][1], asbf(xr[6][1]), cb[1][1]);
    SF();
    pw3[0][3] = wfrag(L + 114688 + pa[3]);
    SF();
    cb[0][2] = MFMA16(pw3[1][2], asbf(xr[6][0]), cb[0][2]);
    SF();
    cb[1][2] = MFMA16(pw3[1][2], asbf(xr[6][1]), cb[1][2]);
    SF();
    cb[0][3] = MFMA16(pw3[1][3], asbf(xr[6][0]), cb[0][3]);
    SF();
    cb[1][3] = MFMA16(pw3[1][3], asbf(xr[6][1]), cb[1][3]);
    SF();
    fl = ld8c(ws, f3off);
    PIN(xr[7][0]); PIN(xr[7][1]);
    cb[0][0] = MFMA16(pw3[0][0], asbf(xr[7][0]), cb[0][0]);
    SF();
    pw3[1][0] = wfrag(L + 131072 + pa[0]);
    SF();
    cb[1][0] = MFMA16(pw3[0][0], asbf(xr[7][1]), cb[1][0]);
    SF();
    pw3[1][1] = wfrag(L + 131072 + pa[1]);
    SF();
    cb[0][1] = MFMA16(pw3[0][1], asbf(xr[7][0]), cb[0][1]);
    SF();
    pw3[1][2] = wfrag(L + 131072 + pa[2]);
    SF();
    cb[1][1] = MFMA16(pw3[0][1], asbf(xr[7][1]), cb[1][1]);
    SF();
    pw3[1][3] = wfrag(L + 131072 + pa[3]);
    SF();
    cb[0][2] = MFMA16(pw3[0][2], asbf(xr[7][0]), cb[0][2]);
    SF();
    cb[1][2] = MFMA16(pw3[0][2], asbf(xr[7][1]), cb[1][2]);
    SF();
    cb[0][3] = MFMA16(pw3[0][3], asbf(xr[7][0]), cb[0][3]);
    SF();
    cb[1][3] = MFMA16(pw3[0][3], asbf(xr[7][1]), cb[1][3]);
    SF();
    WAIT_VM(0);
    PIN(fl);
    spin_flags(ws, f3off, fl, tag);
    STAMP(23);
    XL(11, 0);
    XL(11, 1);
    XL(12, 0);
    XL(12, 1);
    XL(13, 0);
    XL(13, 1);
    XL(14, 0);
    XL(14, 1);
    PIN(xr[8][0]); PIN(xr[8][1]);
    cb[0][0] = MFMA16(pw3[1][0], asbf(xr[8][0]), cb[0][0]);
    SF();
    pw3[0][0] = wfrag(L + 147456 + pa[0]);
    SF();
    cb[1][0] = MFMA16(pw3[1][0], asbf(xr[8][1]), cb[1][0]);
    SF();
    pw3[0][1] = wfrag(L + 147456 + pa[1]);
    SF();
    cb[0][1] = MFMA16(pw3[1][1], asbf(xr[8][0]), cb[0][1]);
    SF();
    pw3[0][2] = wfrag(L + 147456 + pa[2]);
    SF();
    cb[1][1] = MFMA16(pw3[1][1], asbf(xr[8][1]), cb[1][1]);
    SF();
    pw3[0][3] = wfrag(L + 147456 + pa[3]);
    SF();
    cb[0][2] = MFMA16(pw3[1][2], asbf(xr[8][0]), cb[0][2]);
    SF();
    cb[1][2] = MFMA16(pw3[1][2], asbf(xr[8][1]), cb[1][2]);
    SF();
    cb[0][3] = MFMA16(pw3[1][3], asbf(xr[8][0]), cb[0][3]);
    SF();
    cb[1][3] = MFMA16(pw3[1][3], asbf(xr[8][1]), cb[1][3]);
    SF();
    BAR();
    STAMP(22);
    if (blockIdx.x == 0 && wid == 7) {
    #pragma unroll
        for (int i = 0; i < 4; ++i) { PIN(fe[i]); spin_flags(ws, WS_F1 + (unsigned)(lane + 64 * i) * 128, fe[i], tag); }
        __builtin_amdgcn_raw_buffer_store_b64((u32x2){(unsigned)(ep + 1), (unsigned)((ep + 1) >> 32)}, wsr, lane == 0 ? WS_E : DROP, 0, 16);
    }
    PIN(xr[9][0]); PIN(xr[9][1]);
    cb[0][0] = MFMA16(pw3[0][0], asbf(xr[9][0]), cb[0][0]);
    SF();
    pw3[1][0] = wfrag(L + 0 + pa[0]);
    SF();
    cb[1][0] = MFMA16(pw3[0][0], asbf(xr[9][1]), cb[1][0]);
    SF();
    pw3[1][1] = wfrag(L + 0 + pa[1]);
    SF();
    cb[0][1] = MFMA16(pw3[0][1], asbf(xr[9][0]), cb[0][1]);
    SF();
    pw3[1][2] = wfrag(L + 0 + pa[2]);
    SF();
    cb[1][1] = MFMA16(pw3[0][1], asbf(xr[9][1]), cb[1][1]);
    SF();
    pw3[1][3] = wfrag(L + 0 + pa[3]);
    SF();
    cb[0][2] = MFMA16(pw3[0][2], asbf(xr[9][0]), cb[0][2]);
    SF();
    cb[1][2] = MFMA16(pw3[0][2], asbf(xr[9][1]), cb[1][2]);
    SF();
    cb[0][3] = MFMA16(pw3[0][3], asbf(xr[9][0]), cb[0][3]);
    SF();
    cb[1][3] = MFMA16(pw3[0][3], asbf(xr[9][1]), cb[1][3]);
    SF();
    WAIT_VM(6);
    PIN(xr[11][0]); PIN(xr[11][1]);
    ca[0][0] = MFMA16(pw3[1][0], asbf(xr[11][0]), ca[0][0]);
    SF();
    pw3[0][0] = wfrag(L + 16384 + pa[0]);
    SF();
    ca[1][0] = MFMA16(pw3[1][0], asbf(xr[11][1]), ca[1][0]);
    SF();
    pw3[0][1] = wfrag(L + 16384 + pa[1]);
    SF();
    ca[0][1] = MFMA16(pw3[1][1], asbf(xr[11][0]), ca[0][1]);
    SF();
    pw3[0][2] = wfrag(L + 16384 + pa[2]);
    SF();
    ca[1][1] = MFMA16(pw3[1][1], asbf(xr[11][1]), ca[1][1]);
    SF();
    pw3[0][3] = wfrag(L + 16384 + pa[3]);
    SF();
    ca[0][2] = MFMA16(pw3[1][2], asbf(xr[11][0]), ca[0][2]);
    SF();
    ca[1][2] = MFMA16(pw3[1][2], asbf(xr[11][1]), ca[1][2]);
    SF();
    ca[0][3] = MFMA16(pw3[1][3], asbf(xr[11][0]), ca[0][3]);
    SF();
    ca[1][3] = MFMA16(pw3[1][3], asbf(xr[11][1]), ca[1][3]);
    SF();
    WAIT_VM(4);
    PIN(xr[12][0]); PIN(xr[12][1]);
    ca[0][0] = MFMA16(pw3[0][0], asbf(xr[12][0]), ca[0][0]);
    SF();
    pw3[1][0] = wfrag(L + 32768 + pa[0]);
    SF();
    ca[1][0] = MFMA16(pw3[0][0], asbf(xr[12][1]), ca[1][0]);
    SF();
    pw3[1][1] = wfrag(L + 32768 + pa[1]);
    SF();
    ca[0][1] = MFMA16(pw3[0][1], asbf(xr[12][0]), ca[0][1]);
    SF();
    pw3[1][2] = wfrag(L + 32768 + pa[2]);
    SF();
    ca[1][1] = MFMA16(pw3[0][1], asbf(xr[12][1]), ca[1][1]);
    SF();
    pw3[1][3] = wfrag(L + 32768 + pa[3]);
    SF();
    ca[0][2] = MFMA16(pw3[0][2], asbf(xr[12][0]), ca[0][2]);
    SF();
    ca[1][2] = MFMA16(pw3[0][2], asbf(xr[12][1]), ca[1][2]);
    SF();
    ca[0][3] = MFMA16(pw3[0][3], asbf(xr[12][0]), ca[0][3]);
    SF();
    ca[1][3] = MFMA16(pw3[0][3], asbf(xr[12][1]), ca[1][3]);
    SF();
    WAIT_VM(2);
    PIN(xr[13][0]); PIN(xr[13][1]);
    ca[0][0] = MFMA16(pw3[1][0], asbf(xr[13][0]), ca[0][0]);
    SF();
    pw3[0][0] = wfrag(L + 49152 + pa[0]);
    SF();
    ca[1][0] = MFMA16(pw3[1][0], asbf(xr[13][1]), ca[1][0]);
    SF();
    pw3[0][1] = wfrag(L + 49152 + pa[1]);
    SF();
    ca[0][1] = MFMA16(pw3[1][1], asbf(xr[13][0]), ca[0][1]);
    SF();
    pw3[0][2] = wfrag(L + 49152 + pa[2]);
    SF();
    ca[1][1] = MFMA16(pw3[1][1], asbf(xr[13][1]), ca[1][1]);
    SF();
    pw3[0][3] = wfrag(L + 49152 + pa[3]);
    SF();
    ca[0][2] = MFMA16(pw3[1][2], asbf(xr[13][0]), ca[0][2]);
    SF();
    ca[1][2] = MFMA16(pw3[1][2], asbf(xr[13][1]), ca[1][2]);
    SF();
    ca[0][3] = MFMA16(pw3[1][3], asbf(xr[13][0]), ca[0][3]);
    SF();
    ca[1][3] = MFMA16(pw3[1][3], asbf(xr[13][1]), ca[1][3]);
    SF();
    WAIT_VM(0);
    PIN(xr[14][0]); PIN(xr[14][1]);
    ca[0][0] = MFMA16(pw3[0][0], asbf(xr[14][0]), ca[0][0]);
    SF();
    ca[1][0] = MFMA16(pw3[0][0], asbf(xr[14][1]), ca[1][0]);
    SF();
    ca[0][1] = MFMA16(pw3[0][1], asbf(xr[14][0]), ca[0][1]);
    SF();
    ca[1][1] = MFMA16(pw3[0][1], asbf(xr[14][1]), ca[1][1]);
    SF();
    ca[0][2] = MFMA16(pw3[0][2], asbf(xr[14][0]), ca[0][2]);
    SF();
    ca[1][2] = MFMA16(pw3[0][2], asbf(xr[14][1]), ca[1][2]);
    SF();
    ca[0][3] = MFMA16(pw3[0][3], asbf(xr[14][0]), ca[0][3]);
    SF();
    ca[1][3] = MFMA16(pw3[0][3], asbf(xr[14][1]), ca[1][3]);
    SF();
    STAMP(24);
    {
        f32x4 c[2][4];
#pragma unroll
        for (int a = 0; a < 2; ++a)
#pragma unroll
            for (int b = 0; b < 4; ++b) c[a][b] = ca[a][b] * gg[a] + cb[a][b] * (1.0f - gg[a]);
        char* ex = lds + 4 * HS + lane * 16;
#pragma unroll
        for (int x = 1; x < 4; ++x)
#pragma unroll
            for (int n2 = 0; n2 < 2; ++n2) *(f32x4*)(ex + ((((tp * 4 + (kq ^ x)) * 3 + (x - 1)) * 2 + n2) << 10)) = c[x >> 1][(x & 1) * 2 + n2];
        BAR();
        u32x2 pk[2];
#pragma unroll
        for (int n2 = 0; n2 < 2; ++n2) {
            f32x4 v = c[0][n2];
#pragma unroll
            for (int x = 1; x < 4; ++x) v += *(const f32x4*)(ex + ((((tp * 4 + kq) * 3 + (x - 1)) * 2 + n2) << 10));
            pk[n2] = (u32x2){cvtpk(v[0], v[1]), cvtpk(v[2], v[3])};
        }
        __builtin_amdgcn_raw_buffer_store_b128(pair16(pk[0], pk[1]), __builtin_amdgcn_make_buffer_rsrc(out, 0, 2048 * 1024, 0x00020000),
            (unsigned)((mt * 64 + tp * 32 + (kq >> 1) * 16 + fr) * 1024 + (nt * 64 + (kq & 1) * 32 + (g & 1) * 16 + (g >> 1) * 8) * 2), 0, 2);
    }
    STAMP(25);

}

extern "C" void kernel_launch(void* const* d_in, const int* in_sizes, int n_in,
                              void* d_out, int out_size, void* d_ws, size_t ws_size,
                              hipStream_t stream) {
    (void)hipFuncSetAttribute((const void*)k_fused, hipFuncAttributeMaxDynamicSharedMemorySize, 163840);
    k_fused<<<256, 512, 163840, stream>>>((const char*)d_in[0], (const char*)d_in[1], (const char*)d_in[2], (const char*)d_in[4],
                                          (const char*)d_in[5], (const char*)d_in[6], (char*)d_out, (char*)d_ws);
}
```
